```python
import torch

def _softmax(x, axis=-1):
    return torch.softmax(x, dim=axis)
def _gelu(x):
    return torch.nn.functional.gelu(x, approximate="tanh")
def _silu(x):
    return torch.nn.functional.silu(x)
def _sigmoid(x):
    return torch.sigmoid(x)
def _layernorm(x, axis=-1, eps=1e-5):
    m = x.mean(dim=axis, keepdim=True)
    v = x.var(dim=axis, keepdim=True, unbiased=False)
    return (x - m) / torch.sqrt(v + eps)
def _scatter_add(tgt, idx, src, axis=0):
    return tgt.clone().index_add_(axis, idx, src)
def _segment_sum(src, seg, n):
    out = torch.zeros((n,) + src.shape[1:], dtype=src.dtype)
    return out.index_add_(0, seg, src)
def _pad(x, pw):
    flat = tuple(v for pair in reversed(pw) for v in pair)
    return torch.nn.functional.pad(x, flat)

kernel_name = 'parallel_sum__sliding_block_cached__dense_activation_relu__decode'

def setup_inputs():

    _w = (lambda a: (lambda t: t.to(torch.bfloat16) if t.is_floating_point() else t)(torch.as_tensor(a)))
    def _A_setup():
        B, Sq, D = 1, 1, 512
        Hq, Hkv, Sk, Dh = 32, 8, 65536, 128
        assert Hq % Hkv == 0, (Hq, Hkv)
        return {
            "x": _w((lambda *s: torch.randn(*s, dtype=torch.bfloat16) * 0.05)(B, Sq, D)),
            "k_cache": _w((lambda *s: torch.randn(*s, dtype=torch.bfloat16) * 3.2)(B, Hkv, Sk, Dh)),
            "v_cache": _w((lambda *s: torch.randn(*s, dtype=torch.bfloat16) * 1.6)(B, Hkv, Sk, Dh)),
            "wq": _w((lambda *s: torch.randn(*s, dtype=torch.bfloat16) * 3.2)(D, Hq * Dh) * (D ** -0.5)),
            "wo": _w((lambda *s: torch.randn(*s, dtype=torch.bfloat16) * 1.6)(Hq * Dh, D) * ((Hq * Dh) ** -0.5)),
        }
    _A_in = _A_setup()

    def _B_setup():
        B, S, D, Dff = 1, 1, 512, 1792
        return {
            "x": _w((lambda *s: torch.randn(*s, dtype=torch.bfloat16) * 0.05)(B, S, D)),
            "w1": _w((lambda *s: torch.randn(*s, dtype=torch.bfloat16) * 0.05)(D, Dff)),
            "w2": _w((lambda *s: torch.randn(*s, dtype=torch.bfloat16) * 0.05)(Dff, D)),
        }
    _B_in = _B_setup()

    return {
        "A_x": _A_in["x"],
        "A_k_cache": _A_in["k_cache"],
        "A_v_cache": _A_in["v_cache"],
        "A_wq": _A_in["wq"],
        "A_wo": _A_in["wo"],
        "B_x": _B_in["x"],
        "B_w1": _B_in["w1"],
        "B_w2": _B_in["w2"],
    }

def reference(A_x, A_k_cache, A_v_cache, A_wq, A_wo, B_x, B_w1, B_w2):
    def _A_ref(x, k_cache, v_cache, wq, wo):
        B, Sq, D = x.shape
        Hq, Dh = 32, 128
        Hkv, Sk = k_cache.shape[1], k_cache.shape[2]
        W = min(4096, max(256, Sk // 4))
        rep = Hq // Hkv
        q = (lambda x, *d: x.permute(*d))((x @ wq).reshape(B, Sq, Hq, Dh), 0, 2, 1, 3)
        k = (lambda x, n, axis: torch.repeat_interleave(x, n, dim=axis))(k_cache[:, :, Sk - W:, :], rep, axis=1)
        v = (lambda x, n, axis: torch.repeat_interleave(x, n, dim=axis))(v_cache[:, :, Sk - W:, :], rep, axis=1)
        scores = torch.einsum("bhqd,bhkd->bhqk", q, k) * (Dh ** -0.5)
        p = _softmax(scores, axis=-1)
        o = torch.einsum("bhqk,bhkd->bhqd", p, v)
        return (lambda x, *d: x.permute(*d))(o, 0, 2, 1, 3).reshape(B, Sq, Hq * Dh) @ wo

    def _B_ref(x, w1, w2):
        h = torch.einsum("bsd,df->bsf", x, w1)
        zero = torch.zeros_like(h)
        h = torch.maximum(h, zero)
        return torch.einsum("bsf,fd->bsd", h, w2)

    x = A_x
    xn = _layernorm(x, axis=-1)
    return x + _A_ref(xn, A_k_cache, A_v_cache, A_wq, A_wo) + _B_ref(xn, B_w1, B_w2)
```

```cpp
#include <hip/hip_runtime.h>
#include <hip/hip_bf16.h>
#include <stdint.h>

#define MIMIC 1

typedef unsigned short u16;
typedef unsigned v4u __attribute__((ext_vector_type(4)));

__device__ __forceinline__ float bf2f(unsigned h) { return __uint_as_float(h << 16); }
__device__ __forceinline__ float rbf(float f) {
    return __bfloat162float(__float2bfloat16(f));
}
__device__ __forceinline__ float mim(float f) {
    return rbf(f);
}

constexpr int D = 512, HQ = 32, HKV = 8, SK = 65536, DH = 128, W = 4096, DFF = 1792;
constexpr int NQ = HQ * DH;
constexpr int NSPLIT = 32;
constexpr int KEYS = W / NSPLIT;

constexpr int WS_Q = 0, WS_H = 4096, WS_P = 8192;

__device__ __forceinline__ void compute_xn(const u16* x, float* xn  , float* red  , int tid, int nthr) {
    float a = bf2f(x[tid]), b = bf2f(x[tid + 256]);
    float s = a + b;
    for (int o = 32; o >= 1; o >>= 1) s += __shfl_xor(s, o);
    if ((tid & 63) == 0) red[tid >> 6] = s;
    __syncthreads();
    float mean = (red[0] + red[1] + red[2] + red[3]) * (1.f / 512.f);
    __syncthreads();
    float da = a - mean, db = b - mean;
    float v = da * da + db * db;
    for (int o = 32; o >= 1; o >>= 1) v += __shfl_xor(v, o);
    if ((tid & 63) == 0) red[tid >> 6] = v;
    __syncthreads();
    float var = (red[0] + red[1] + red[2] + red[3]) * (1.f / 512.f);
    float m = mim(mean);
    var = mim(var);
    float den = mim(sqrtf(mim(var + 1e-5f)));
    xn[tid] = mim(mim(a - m) / den);
    xn[tid + 256] = mim(mim(b - m) / den);
    __syncthreads();
}

__global__ void __launch_bounds__(256) k_proj(const u16* x, const u16* wq, const u16* w1, float* ws) {
    __shared__ float xn[512];
    __shared__ float red[8];
    __shared__ float part[64][33];
    int tid = threadIdx.x;
    compute_xn(x, xn, red, tid, 256);
    int cb = blockIdx.x * 32;
    const u16* Wp; int ld; int col; bool isq = cb < NQ;
    if (isq) { Wp = wq; ld = NQ; col = cb; } else { Wp = w1; ld = DFF; col = cb - NQ; }
    int c4 = tid & 3, rg = tid >> 2;
    float acc[8] = {0, 0, 0, 0, 0, 0, 0, 0};
    for (int r = 0; r < 8; ++r) {
        int row = rg * 8 + r;
        v4u w = *(const v4u*)(Wp + (size_t)row * ld + col + c4 * 8);
        float xv = xn[row];
        for (int j = 0; j < 4; ++j) {
            acc[2 * j] += xv * bf2f(w[j] & 0xffff);
            acc[2 * j + 1] += xv * bf2f(w[j] >> 16);
        }
    }
    for (int j = 0; j < 8; ++j) part[rg][c4 * 8 + j] = acc[j];
    __syncthreads();
    if (tid < 32) {
        float s = 0;
        for (int r = 0; r < 64; ++r) s += part[r][tid];
        s = mim(s);
        if (isq) ws[WS_Q + col + tid] = s; else ws[WS_H + col + tid] = fmaxf(s, 0.f);
    }
}

__global__ void __launch_bounds__(256) k_attn(const u16* kc, const u16* vc, float* ws) {
    __shared__ float q[4][128];
    __shared__ float sc[4][KEYS];
    __shared__ float red[4][4];
    __shared__ float oacc[4][4][128];
    int tid = threadIdx.x, s = blockIdx.x, g = blockIdx.y;
    for (int i = tid; i < 512; i += 256) q[i >> 7][i & 127] = ws[WS_Q + g * 512 + i];
    __syncthreads();
    const u16* kb = kc + ((size_t)g * SK + (SK - W) + s * KEYS) * DH;
    const u16* vb = vc + ((size_t)g * SK + (SK - W) + s * KEYS) * DH;
    int c = tid & 15, kr = tid >> 4;
    for (int it = 0; it < KEYS / 16; ++it) {
        int key = it * 16 + kr;
        v4u kv = *(const v4u*)(kb + (size_t)key * DH + c * 8);
        float kf[8];
        for (int j = 0; j < 4; ++j) { kf[2 * j] = bf2f(kv[j] & 0xffff); kf[2 * j + 1] = bf2f(kv[j] >> 16); }
        for (int h = 0; h < 4; ++h) {
            float d = 0;
            for (int j = 0; j < 8; ++j) d += kf[j] * q[h][c * 8 + j];
            for (int o = 8; o >= 1; o >>= 1) d += __shfl_xor(d, o);
            if (c == 0) sc[h][key] = mim(mim(d) * 0.08838834764831845f);
        }
    }
    __syncthreads();
    int wv = tid >> 6, ln = tid & 63;
    float a = sc[wv][ln], b = sc[wv][ln + 64];
    float m = fmaxf(a, b);
    for (int o = 32; o >= 1; o >>= 1) m = fmaxf(m, __shfl_xor(m, o));
    float pa = __expf(a - m), pb = __expf(b - m);
    float l = pa + pb;
    for (int o = 32; o >= 1; o >>= 1) l += __shfl_xor(l, o);
    sc[wv][ln] = pa; sc[wv][ln + 64] = pb;
    if (ln == 0) { red[wv][0] = m; red[wv][1] = l; }
    __syncthreads();
    float acc[4][8];
    for (int h = 0; h < 4; ++h) for (int j = 0; j < 8; ++j) acc[h][j] = 0;
    for (int it = 0; it < KEYS / 16; ++it) {
        int key = it * 16 + kr;
        v4u vv = *(const v4u*)(vb + (size_t)key * DH + c * 8);
        float vf[8];
        for (int j = 0; j < 4; ++j) { vf[2 * j] = bf2f(vv[j] & 0xffff); vf[2 * j + 1] = bf2f(vv[j] >> 16); }
        for (int h = 0; h < 4; ++h) { float p = sc[h][key]; for (int j = 0; j < 8; ++j) acc[h][j] += p * vf[j]; }
    }
    for (int h = 0; h < 4; ++h) for (int j = 0; j < 8; ++j) {
        float t = acc[h][j];
        t += __shfl_xor(t, 16); t += __shfl_xor(t, 32);
        acc[h][j] = t;
    }
    if (ln < 16) for (int h = 0; h < 4; ++h) for (int j = 0; j < 8; ++j) oacc[wv][h][c * 8 + j] = acc[h][j];
    __syncthreads();
    for (int i = tid; i < 512; i += 256) {
        int h = i >> 7, d = i & 127;
        float t = oacc[0][h][d] + oacc[1][h][d] + oacc[2][h][d] + oacc[3][h][d];
        float* p = ws + WS_P + ((size_t)(g * 4 + h) * NSPLIT + s) * 130;
        p[2 + d] = t;
        if (d == 0) { p[0] = red[h][0]; p[1] = red[h][1]; }
    }
}

__global__ void __launch_bounds__(256) k_out(const u16* x, const u16* wo, const u16* w2, const float* ws, u16* out) {
    __shared__ float o[4096 + DFF];
    __shared__ float part[64][33];
    int tid = threadIdx.x;
    for (int i = tid; i < 4096; i += 256) {
        int h = i >> 7, d = i & 127;
        const float* p = ws + WS_P + (size_t)h * NSPLIT * 130;
        float M = -INFINITY;
        for (int s = 0; s < NSPLIT; ++s) M = fmaxf(M, p[s * 130]);
        float L = 0, acc = 0;
        for (int s = 0; s < NSPLIT; ++s) { float w = __expf(p[s * 130] - M); L += w * p[s * 130 + 1]; acc += w * p[s * 130 + 2 + d]; }
        o[i] = mim(acc / L);
    }
    for (int i = tid; i < DFF; i += 256) o[4096 + i] = ws[WS_H + i];
    __syncthreads();
    int col = blockIdx.x * 32, c4 = tid & 3, rg = tid >> 2;
    float accA[8] = {0, 0, 0, 0, 0, 0, 0, 0}, accB[8] = {0, 0, 0, 0, 0, 0, 0, 0};
    for (int row = rg; row < 4096; row += 64) {
        v4u w = *(const v4u*)(wo + (size_t)row * D + col + c4 * 8);
        float xv = o[row];
        for (int j = 0; j < 4; ++j) { accA[2 * j] += xv * bf2f(w[j] & 0xffff); accA[2 * j + 1] += xv * bf2f(w[j] >> 16); }
    }
    for (int row = rg; row < DFF; row += 64) {
        v4u w = *(const v4u*)(w2 + (size_t)row * D + col + c4 * 8);
        float xv = o[4096 + row];
        for (int j = 0; j < 4; ++j) { accB[2 * j] += xv * bf2f(w[j] & 0xffff); accB[2 * j + 1] += xv * bf2f(w[j] >> 16); }
    }
    for (int j = 0; j < 8; ++j) part[rg][c4 * 8 + j] = accA[j];
    __syncthreads();
    float sa = 0, sb = 0;
    if (tid < 32) for (int r = 0; r < 64; ++r) sa += part[r][tid];
    __syncthreads();
    for (int j = 0; j < 8; ++j) part[rg][c4 * 8 + j] = accB[j];
    __syncthreads();
    if (tid < 32) {
        for (int r = 0; r < 64; ++r) sb += part[r][tid];
        float xv = bf2f(x[col + tid]);
        float r = mim(xv + mim(sa)) + mim(sb);
        __hip_bfloat16 o16 = __float2bfloat16(r);
        out[col + tid] = *(u16*)&o16;
    }
}

extern "C" void kernel_launch(void* const* d_in, const int* in_sizes, int n_in,
                              void* d_out, int out_size, void* d_ws, size_t ws_size,
                              hipStream_t stream) {
    const u16* x = (const u16*)d_in[0];
    const u16* kc = (const u16*)d_in[1];
    const u16* vc = (const u16*)d_in[2];
    const u16* wq = (const u16*)d_in[3];
    const u16* wo = (const u16*)d_in[4];
    const u16* w1 = (const u16*)d_in[6];
    const u16* w2 = (const u16*)d_in[7];
    float* ws = (float*)d_ws;
    k_proj<<<(NQ + DFF) / 32, 256, 0, stream>>>(x, wq, w1, ws);
    k_attn<<<dim3(NSPLIT, HKV), 256, 0, stream>>>(kc, vc, ws);
    k_out<<<16, 256, 0, stream>>>(x, wo, w2, ws, (u16*)d_out);
}
```

```cpp
#include <hip/hip_runtime.h>
#include <hip/hip_bf16.h>
#include <stdint.h>

typedef unsigned short u16;
typedef unsigned v4u __attribute__((ext_vector_type(4)));
typedef unsigned v2u __attribute__((ext_vector_type(2)));
typedef float v4f __attribute__((ext_vector_type(4)));
typedef float v2f __attribute__((ext_vector_type(2)));
typedef short v4s __attribute__((ext_vector_type(4)));
typedef __bf16 bf16x8 __attribute__((ext_vector_type(8)));

#define EARLY_V 0
#define NT_LOADS 1

constexpr int D = 512, HKV = 8, SK = 65536, DH = 128, WIN = 4096, DFF = 1792, NQ = 4096;
constexpr int NWG = 256, NCW = 8, NTHR = (NCW + 1) * 64;
constexpr unsigned WS_G = 0;
constexpr unsigned WS_QG = 4096;
constexpr unsigned WS_HG = WS_QG + 16384 * 8;
constexpr unsigned WS_ML = WS_HG + 16384 * 8;
constexpr unsigned WS_P2 = WS_ML + 2048 * 8;
constexpr unsigned WS_P3 = WS_P2 + 131072 * 8;
constexpr unsigned WS_END = WS_P3 + 16384 * 8;
constexpr unsigned WS_STAMP = 4u << 20;
constexpr unsigned OOB = 0xfffffff0u;
constexpr int L_PART = 0;
constexpr int L_T = 4096;
constexpr int L_QS = 4352;
constexpr int L_ML = 5632;
constexpr int L_OW = 8192;
constexpr int LDS_BYTES = 84 * 1024;
constexpr unsigned SPIN_CAP = 1u << 18;

__device__ __forceinline__ float bf2f(unsigned h) { return __uint_as_float(h << 16); }
__device__ __forceinline__ float bflo(unsigned w) { return __uint_as_float(w << 16); }
__device__ __forceinline__ float bfhi(unsigned w) { return __uint_as_float(w & 0xffff0000u); }
__device__ __forceinline__ unsigned f2bf(float f) { __hip_bfloat16 h = __float2bfloat16(f); return (unsigned)__builtin_bit_cast(unsigned short, h); }
__device__ __forceinline__ float rbf(float f) { return bf2f(f2bf(f)); }
__device__ __forceinline__ float u2f(unsigned u) { return __uint_as_float(u); }
__device__ __forceinline__ unsigned f2u(float f) { return __float_as_uint(f); }
template <int CTRL> __device__ __forceinline__ float dpp(float x) {
    return __builtin_bit_cast(float, __builtin_amdgcn_mov_dpp(__builtin_bit_cast(int, x), CTRL, 0xf, 0xf, true));
}
constexpr int XOR1 = 0xB1, XOR2 = 0x4E, XOR7 = 0x141, XOR8 = 0x128, XOR15 = 0x140;
__device__ __forceinline__ float xrow_sum(float x) {
    auto s = __builtin_amdgcn_permlane16_swap(f2u(x), f2u(x), false, false);
    x = u2f(s[0]) + u2f(s[1]);
    auto t = __builtin_amdgcn_permlane32_swap(f2u(x), f2u(x), false, false);
    return u2f(t[0]) + u2f(t[1]);
}
__device__ __forceinline__ float xrow_max(float x) {
    auto s = __builtin_amdgcn_permlane16_swap(f2u(x), f2u(x), false, false);
    x = fmaxf(u2f(s[0]), u2f(s[1]));
    auto t = __builtin_amdgcn_permlane32_swap(f2u(x), f2u(x), false, false);
    return fmaxf(u2f(t[0]), u2f(t[1]));
}
__device__ __forceinline__ float wave_sum(float x) {
    x += dpp<XOR1>(x); x += dpp<XOR2>(x); x += dpp<XOR7>(x); x += dpp<XOR15>(x);
    return xrow_sum(x);
}
__device__ __forceinline__ float tr_reduce8(const float (&y)[8], int lane) {
    float a[4];
#pragma unroll
    for (int i = 0; i < 4; ++i) { const float t1 = y[i] + dpp<XOR8>(y[i]), t2 = y[i + 4] + dpp<XOR8>(y[i + 4]); a[i] = (lane & 8) ? t2 : t1; }
    float z[2];
#pragma unroll
    for (int i = 0; i < 2; ++i) { auto r = __builtin_amdgcn_permlane16_swap(f2u(a[i]), f2u(a[i + 2]), false, false); z[i] = u2f(r[0]) + u2f(r[1]); }
    auto r = __builtin_amdgcn_permlane32_swap(f2u(z[0]), f2u(z[1]), false, false);
    return u2f(r[0]) + u2f(r[1]);
}
__device__ __forceinline__ float sum_hi3(float x) { x += dpp<XOR8>(x); return xrow_sum(x); }
__device__ __forceinline__ float max_hi3(float x) { x = fmaxf(x, dpp<XOR8>(x)); return xrow_max(x); }
__device__ __forceinline__ v4u ldw(const u16* p) {
    return __builtin_nontemporal_load((const v4u*)p);
}
#define BAR() do { asm volatile("s_waitcnt lgkmcnt(0)" ::: "memory"); __builtin_amdgcn_s_barrier(); asm volatile("" ::: "memory"); } while (0)
#define LD128(off) __builtin_amdgcn_raw_buffer_load_b128(rs, (off), 0, 16)
#define LD64(off) __builtin_amdgcn_raw_buffer_load_b64(rs, (off), 0, 16)
#define ST128(v, off) __builtin_amdgcn_raw_buffer_store_b128((v), rs, (off), 0, 16)
#define ST64(v, off) __builtin_amdgcn_raw_buffer_store_b64((v), rs, (off), 0, 16)

#define STAMP(i) do { } while (0)

__global__ void __launch_bounds__(NTHR) fused_decode(const u16* __restrict__ x, const u16* __restrict__ kc, const u16* __restrict__ vc,
                                                     const u16* __restrict__ wq, const u16* __restrict__ wo, const u16* __restrict__ w1,
                                                     const u16* __restrict__ w2, u16* __restrict__ out, unsigned char* ws) {
    __shared__ __attribute__((aligned(16))) unsigned char smem[LDS_BYTES];
    const int b = blockIdx.x, g = b & 7, s = b >> 3;
    const int tid = threadIdx.x, lane = tid & 63, wave = __builtin_amdgcn_readfirstlane(tid >> 6);
    const bool has1 = s < 28;
    const int cb = s >> 2, kq = s & 3;
    const int hh3 = g * 4 + (s >> 3), cg3 = s & 7;
    const __amdgpu_buffer_rsrc_t rs = __builtin_amdgcn_make_buffer_rsrc(ws, 0, WS_END, 0x00020000);
    const int c = lane & 15, quad = lane >> 4;
    const int r3 = lane >> 3, c3 = lane & 7;
    float* part = (float*)(smem + L_PART);

    if (wave < NCW) {
        if (wave == 0) STAMP(0);
        const int prow = tid >> 3;
        const v4u xv = *(const v4u*)(x + lane * 8);
        const unsigned xq0 = x[kq * 128 + prow], xq1 = x[kq * 128 + 64 + prow], xh = x[g * 64 + prow];
        const u16* wqp = wq + (size_t)(kq * 128 + prow) * NQ + g * 512 + cb * 64 + c3 * 8;
        const v4u wq0 = ldw(wqp), wq1 = ldw(wqp + (size_t)64 * NQ);
        v4u w1v = {0u, 0u, 0u, 0u};
        if (has1) w1v = ldw(w1 + (size_t)(g * 64 + prow) * DFF + s * 64 + c3 * 8);
        const size_t kvbase = ((size_t)g * SK + (SK - WIN) + s * 128 + wave * 16) * DH;
        const u16* kp = kc + kvbase + (size_t)c * DH + quad * 8;
        v4u kf[4];
#pragma unroll
        for (int kk = 0; kk < 4; ++kk) kf[kk] = ldw(kp + 32 * kk);
        const u16* vp = vc + kvbase + (size_t)(quad * 4) * DH + c * 8;
        const u16* wop = wo + (size_t)(hh3 * 128 + wave * 16 + r3) * D + cg3 * 64 + c3 * 8;
        const u16* w2p = w2 + (size_t)(hh3 * 56 + wave * 7 + (r3 < 7 ? r3 : 6)) * D + cg3 * 64 + c3 * 8;
        v4u vf[4], wo0, wo1, w2v;
        __builtin_amdgcn_sched_barrier(0);
        float xe[8];
#pragma unroll
        for (int i = 0; i < 4; ++i) { xe[2 * i] = bflo(xv[i]); xe[2 * i + 1] = bfhi(xv[i]); }
        float sm = ((xe[0] + xe[1]) + (xe[2] + xe[3])) + ((xe[4] + xe[5]) + (xe[6] + xe[7]));
        const float mean = wave_sum(sm) * (1.f / 512.f);
        float sq = 0.f;
#pragma unroll
        for (int i = 0; i < 8; ++i) { const float d = xe[i] - mean; sq = fmaf(d, d, sq); }
        const float var = wave_sum(sq) * (1.f / 512.f);
        const float den = rbf(sqrtf(rbf(rbf(var) + 1e-5f)));
        const float mb = rbf(mean);
        const float xn0 = rbf(rbf(bf2f(xq0) - mb) / den), xn1 = rbf(rbf(bf2f(xq1) - mb) / den), xnh = rbf(rbf(bf2f(xh) - mb) / den);
        {
            float a[8], h8[8];
#pragma unroll
            for (int i = 0; i < 4; ++i) {
                a[2 * i] = fmaf(xn1, bflo(wq1[i]), xn0 * bflo(wq0[i])); a[2 * i + 1] = fmaf(xn1, bfhi(wq1[i]), xn0 * bfhi(wq0[i]));
                h8[2 * i] = xnh * bflo(w1v[i]); h8[2 * i + 1] = xnh * bfhi(w1v[i]);
            }
            const float qa = tr_reduce8(a, lane), ha = tr_reduce8(h8, lane);
            const int col = 8 * c3 + ((lane >> 1) & 4) + ((lane >> 3) & 2) + (lane >> 5);
            part[wave * 128 + col] = qa; part[wave * 128 + 64 + col] = ha;
        }
        if (wave == 0) STAMP(1);
        BAR();
#pragma unroll
        for (int jj = 0; jj < 4; ++jj) vf[jj] = ldw(vp + jj * DH);
        wo0 = ldw(wop); wo1 = ldw(wop + 8 * D); w2v = ldw(w2p);
        __builtin_amdgcn_sched_barrier(0);
        BAR();
        if (wave == 0) STAMP(2);
        const unsigned T = *(const unsigned*)(smem + L_T);
        v4f sc = {0.f, 0.f, 0.f, 0.f};
#pragma unroll
        for (int kk = 0; kk < 4; ++kk) {
            v4u qv = *(const v4u*)(smem + L_QS + ((c & 3) * 128 + 32 * kk + 8 * quad) * 2);
            if (c >= 4) qv = (v4u){0u, 0u, 0u, 0u};
            sc = __builtin_amdgcn_mfma_f32_16x16x32_bf16(__builtin_bit_cast(bf16x8, kf[kk]), __builtin_bit_cast(bf16x8, qv), sc, 0, 0, 0);
        }
        float sv[4];
#pragma unroll
        for (int r = 0; r < 4; ++r) sv[r] = rbf(rbf(sc[r]) * 0.08838834764831845f);
        const float mw = xrow_max(fmaxf(fmaxf(sv[0], sv[1]), fmaxf(sv[2], sv[3])));
        float* ml = (float*)(smem + L_ML);
        {
            const float lw = xrow_sum((__expf(sv[0] - mw) + __expf(sv[1] - mw)) + (__expf(sv[2] - mw) + __expf(sv[3] - mw)));
            if (lane < 4) *(v2f*)(ml + (wave * 4 + lane) * 2) = (v2f){mw, lw};
        }
        BAR();
        float M = -INFINITY;
#pragma unroll
        for (int w = 0; w < 8; ++w) M = fmaxf(M, ml[(w * 4 + (c & 3)) * 2]);
        const v2u pa = {f2bf(__expf(sv[0] - M)) | (f2bf(__expf(sv[1] - M)) << 16), f2bf(__expf(sv[2] - M)) | (f2bf(__expf(sv[3] - M)) << 16)};
        if (wave == 0) STAMP(3);
        __builtin_amdgcn_sched_barrier(0);
        v4f o[8];
#pragma unroll
        for (int i = 0; i < 8; ++i) {
            const unsigned sel = (i & 1) ? 0x07060302u : 0x05040100u;
            const v2u bfr = {__builtin_amdgcn_perm(vf[1][i >> 1], vf[0][i >> 1], sel), __builtin_amdgcn_perm(vf[3][i >> 1], vf[2][i >> 1], sel)};
            o[i] = __builtin_amdgcn_mfma_f32_16x16x16bf16_1k(__builtin_bit_cast(v4s, pa), __builtin_bit_cast(v4s, bfr), (v4f){0.f, 0.f, 0.f, 0.f}, 0, 0, 0);
        }
        if (wave == 0) STAMP(4);
        float* ow = (float*)(smem + L_OW);
        if (quad == 0) {
#pragma unroll
            for (int h = 0; h < 4; ++h) {
                *(v4f*)(ow + (wave * 4 + h) * 128 + 8 * c) = (v4f){o[0][h], o[1][h], o[2][h], o[3][h]};
                *(v4f*)(ow + (wave * 4 + h) * 128 + 8 * c + 4) = (v4f){o[4][h], o[5][h], o[6][h], o[7][h]};
            }
        }
        BAR();
        {
            const int h = wave >> 1, d = (wave & 1) * 64 + lane;
            float o0 = 0.f;
#pragma unroll
            for (int w = 0; w < 8; ++w) o0 += ow[(w * 4 + h) * 128 + d];
            ST64(((v2u){f2u(o0), T}), WS_P2 + (unsigned)((((g * 4 + h) * 8 + (d >> 4)) * 32 + s) * 16 + (d & 15)) * 8u);
        }
        if (wave == 0) STAMP(5);
        float oa, ob, hv;
        {
            const unsigned pbase = WS_P2 + (unsigned)(hh3 * 8 + wave) * 4096u + lane * 16u;
            const unsigned mbase = WS_ML + (unsigned)(hh3 * 32 + r3) * 16u;
            const unsigned hoff = WS_HG + (unsigned)(r3 * 2048 + hh3 * 56 + wave * 7 + (c3 < 7 ? c3 : 6)) * 8u;
            v4u pv[4], mv[4]; v2u hg;
            bool okp[4] = {false, false, false, false}, okm[4] = {false, false, false, false}, okh = false;
            for (unsigned spins = 0;; ++spins) {
                asm volatile("" ::: "memory");
                v4u tp[4], tm[4];
#pragma unroll
                for (int i = 0; i < 4; ++i) { tp[i] = LD128(okp[i] ? OOB : pbase + i * 1024u); tm[i] = LD128(okm[i] ? OOB : mbase + i * 128u); }
                const v2u th = LD64(okh ? OOB : hoff);
                bool ok = true;
#pragma unroll
                for (int i = 0; i < 4; ++i) {
                    if (!okp[i]) { pv[i] = tp[i]; okp[i] = (tp[i][1] == T) & (tp[i][3] == T); }
                    if (!okm[i]) { mv[i] = tm[i]; okm[i] = (tm[i][1] == T) & (tm[i][3] == T); }
                    ok &= okp[i] & okm[i];
                }
                if (!okh) { hg = th; okh = th[1] == T; }
                ok &= okh;
                if (__all(ok) || spins > SPIN_CAP) break;
            }
            const float MM = max_hi3(fmaxf(fmaxf(u2f(mv[0][0]), u2f(mv[1][0])), fmaxf(u2f(mv[2][0]), u2f(mv[3][0]))));
            float L = 0.f, o0 = 0.f, o1 = 0.f;
#pragma unroll
            for (int i = 0; i < 4; ++i) { const float e = __expf(u2f(mv[i][0]) - MM); L = fmaf(e, u2f(mv[i][2]), L); o0 = fmaf(e, u2f(pv[i][0]), o0); o1 = fmaf(e, u2f(pv[i][2]), o1); }
            L = sum_hi3(L); o0 = rbf(sum_hi3(o0) / L); o1 = rbf(sum_hi3(o1) / L);
            const float hs = fmaxf(rbf(sum_hi3(u2f(hg[0]))), 0.f);
            const float t0 = __shfl(o0, r3 >> 1), t1 = __shfl(o1, r3 >> 1), t2 = __shfl(o0, 4 + (r3 >> 1)), t3 = __shfl(o1, 4 + (r3 >> 1));
            oa = (r3 & 1) ? t1 : t0; ob = (r3 & 1) ? t3 : t2;
            hv = __shfl(hs, r3); if (r3 == 7) hv = 0.f;
        }
        if (wave == 0) STAMP(6);
        {
            float y[8];
#pragma unroll
            for (int i = 0; i < 4; ++i) {
                y[2 * i] = fmaf(hv, bflo(w2v[i]), fmaf(ob, bflo(wo1[i]), oa * bflo(wo0[i])));
                y[2 * i + 1] = fmaf(hv, bfhi(w2v[i]), fmaf(ob, bfhi(wo1[i]), oa * bfhi(wo0[i])));
            }
            const float ya = tr_reduce8(y, lane);
            const int col = 8 * c3 + ((lane >> 1) & 4) + ((lane >> 3) & 2) + (lane >> 5);
            part[wave * 128 + col] = ya;
        }
        BAR();
        if (wave == 0) {
            float y = 0.f;
#pragma unroll
            for (int w = 0; w < 8; ++w) y += part[w * 128 + lane];
            ST64(((v2u){f2u(y), T}), WS_P3 + (unsigned)(((cg3 * 4 + (lane >> 4)) * 32 + hh3) * 16 + (lane & 15)) * 8u);
            STAMP(7);
        }
        if (wave == 1 && b < 32) {
            const unsigned xo = *(const unsigned*)(x + 16 * b + 2 * c3);
            v4u pv[4]; bool okp[4] = {false, false, false, false};
            for (unsigned spins = 0;; ++spins) {
                asm volatile("" ::: "memory");
                v4u tp[4]; bool ok = true;
#pragma unroll
                for (int i = 0; i < 4; ++i) tp[i] = LD128(okp[i] ? OOB : WS_P3 + (unsigned)b * 4096u + i * 1024u + lane * 16u);
#pragma unroll
                for (int i = 0; i < 4; ++i) { if (!okp[i]) { pv[i] = tp[i]; okp[i] = (tp[i][1] == T) & (tp[i][3] == T); } ok &= okp[i]; }
                if (__all(ok) || spins > SPIN_CAP) break;
            }
            const float y0 = sum_hi3((u2f(pv[0][0]) + u2f(pv[1][0])) + (u2f(pv[2][0]) + u2f(pv[3][0])));
            const float y1 = sum_hi3((u2f(pv[0][2]) + u2f(pv[1][2])) + (u2f(pv[2][2]) + u2f(pv[3][2])));
            if (lane < 8) *(unsigned*)(out + 16 * b + 2 * lane) = f2bf(bflo(xo) + y0) | (f2bf(bfhi(xo) + y1) << 16);
            if (b == 0 && lane == 0) __builtin_amdgcn_raw_buffer_store_b32(T, rs, WS_G, 0, 16);
            STAMP(13);
        }
    } else {
        STAMP(8);
        const unsigned G = __builtin_amdgcn_raw_buffer_load_b32(rs, WS_G, 0, 16);
        const unsigned T = __builtin_amdgcn_readfirstlane(G) + 1u;
        if (lane == 0) *(unsigned*)(smem + L_T) = T;
        BAR();
        {
            float vq = 0.f, vh = 0.f;
#pragma unroll
            for (int w = 0; w < 8; ++w) { vq += part[w * 128 + lane]; vh += part[w * 128 + 64 + lane]; }
            ST64(((v2u){f2u(vq), T}), WS_QG + (unsigned)((g * 4 + kq) * 512 + cb * 64 + lane) * 8u);
            if (has1) ST64(((v2u){f2u(vh), T}), WS_HG + (unsigned)(g * 2048 + s * 64 + lane) * 8u);
        }
        STAMP(9);
        {
            v4u qv[16]; bool okq[16];
#pragma unroll
            for (int i = 0; i < 16; ++i) okq[i] = false;
            for (unsigned spins = 0;; ++spins) {
                asm volatile("" ::: "memory");
                v4u tq[16]; bool ok = true;
#pragma unroll
                for (int i = 0; i < 16; ++i) tq[i] = LD128(okq[i] ? OOB : WS_QG + (unsigned)g * 16384u + i * 1024u + lane * 16u);
#pragma unroll
                for (int i = 0; i < 16; ++i) { if (!okq[i]) { qv[i] = tq[i]; okq[i] = (tq[i][1] == T) & (tq[i][3] == T); } ok &= okq[i]; }
                if (__all(ok) || spins > SPIN_CAP) break;
            }
#pragma unroll
            for (int h = 0; h < 4; ++h) {
                const float q0 = (u2f(qv[h][0]) + u2f(qv[4 + h][0])) + (u2f(qv[8 + h][0]) + u2f(qv[12 + h][0]));
                const float q1 = (u2f(qv[h][2]) + u2f(qv[4 + h][2])) + (u2f(qv[8 + h][2]) + u2f(qv[12 + h][2]));
                *(unsigned*)(smem + L_QS + (h * 128 + 2 * lane) * 2) = f2bf(q0) | (f2bf(q1) << 16);
            }
        }
        STAMP(10);
        BAR();
        BAR();
        if (lane < 4) {
            const float* ml = (const float*)(smem + L_ML);
            float M = -INFINITY, L = 0.f;
#pragma unroll
            for (int w = 0; w < 8; ++w) M = fmaxf(M, ml[(w * 4 + lane) * 2]);
#pragma unroll
            for (int w = 0; w < 8; ++w) L = fmaf(__expf(ml[(w * 4 + lane) * 2] - M), ml[(w * 4 + lane) * 2 + 1], L);
            ST128(((v4u){f2u(M), T, f2u(L), T}), WS_ML + (unsigned)((g * 4 + lane) * 32 + s) * 16u);
        }
        STAMP(11);
    }
}

extern "C" void kernel_launch(void* const* d_in, const int* in_sizes, int n_in,
                              void* d_out, int out_size, void* d_ws, size_t ws_size,
                              hipStream_t stream) {
    fused_decode<<<NWG, NTHR, 0, stream>>>((const u16*)d_in[0], (const u16*)d_in[1], (const u16*)d_in[2], (const u16*)d_in[3], (const u16*)d_in[4],
                                           (const u16*)d_in[6], (const u16*)d_in[7], (u16*)d_out, (unsigned char*)d_ws);
}
```

```cpp
#include <hip/hip_runtime.h>
#include <hip/hip_bf16.h>
#include <stdint.h>

typedef unsigned short u16;
typedef unsigned v4u __attribute__((ext_vector_type(4)));
typedef unsigned v2u __attribute__((ext_vector_type(2)));
typedef float v4f __attribute__((ext_vector_type(4)));
typedef float v2f __attribute__((ext_vector_type(2)));
typedef short v4s __attribute__((ext_vector_type(4)));
typedef __bf16 bf16x8 __attribute__((ext_vector_type(8)));

#define V_POS 2
#define H2_SLEEP 0
#define WO_POS 1
#define NT_LOADS 1

constexpr int D = 512, HKV = 8, SK = 65536, DH = 128, WIN = 4096, DFF = 1792, NQ = 4096;
constexpr int NWG = 256, NCW = 8, NTHR = NCW * 64;
constexpr unsigned WS_G = 0;
constexpr unsigned WS_QG = 4096;
constexpr unsigned WS_HG = WS_QG + 16384 * 8;
constexpr unsigned WS_ML = WS_HG + 16384 * 8;
constexpr unsigned WS_P2 = WS_ML + 2048 * 8;
constexpr unsigned WS_P3 = WS_P2 + 65536 * 8;
constexpr unsigned WS_END = WS_P3 + 16384 * 8;
constexpr unsigned WS_STAMP = 4u << 20;
constexpr unsigned OOB = 0xfffffff0u;
constexpr int L_PART = 0;
constexpr int L_T = 4096;
constexpr int L_QS = 4352;
constexpr int L_ML = 5632;
constexpr int L_OQ = 6144;
constexpr int L_OW = 8192;
constexpr int LDS_BYTES = 84 * 1024;
constexpr unsigned SPIN_CAP = 1u << 18;

__device__ __forceinline__ float bf2f(unsigned h) { return __uint_as_float(h << 16); }
__device__ __forceinline__ float bflo(unsigned w) { return __uint_as_float(w << 16); }
__device__ __forceinline__ float bfhi(unsigned w) { return __uint_as_float(w & 0xffff0000u); }
__device__ __forceinline__ unsigned f2bf(float f) { __hip_bfloat16 h = __float2bfloat16(f); return (unsigned)__builtin_bit_cast(unsigned short, h); }
__device__ __forceinline__ float rbf(float f) { return bf2f(f2bf(f)); }
__device__ __forceinline__ float u2f(unsigned u) { return __uint_as_float(u); }
__device__ __forceinline__ unsigned f2u(float f) { return __float_as_uint(f); }
template <int CTRL> __device__ __forceinline__ float dpp(float x) {
    return __builtin_bit_cast(float, __builtin_amdgcn_mov_dpp(__builtin_bit_cast(int, x), CTRL, 0xf, 0xf, true));
}
constexpr int XOR1 = 0xB1, XOR2 = 0x4E, XOR7 = 0x141, XOR8 = 0x128, XOR15 = 0x140, ROR4 = 0x124;
__device__ __forceinline__ float xrow_sum(float x) {
    auto s = __builtin_amdgcn_permlane16_swap(f2u(x), f2u(x), false, false);
    x = u2f(s[0]) + u2f(s[1]);
    auto t = __builtin_amdgcn_permlane32_swap(f2u(x), f2u(x), false, false);
    return u2f(t[0]) + u2f(t[1]);
}
__device__ __forceinline__ float xrow_max(float x) {
    auto s = __builtin_amdgcn_permlane16_swap(f2u(x), f2u(x), false, false);
    x = fmaxf(u2f(s[0]), u2f(s[1]));
    auto t = __builtin_amdgcn_permlane32_swap(f2u(x), f2u(x), false, false);
    return fmaxf(u2f(t[0]), u2f(t[1]));
}
__device__ __forceinline__ float wave_sum(float x) {
    x += dpp<XOR1>(x); x += dpp<XOR2>(x); x += dpp<XOR7>(x); x += dpp<XOR15>(x);
    return xrow_sum(x);
}
__device__ __forceinline__ float tr_reduce8(const float (&y)[8], int lane) {
    float a[4];
#pragma unroll
    for (int i = 0; i < 4; ++i) { const float t1 = y[i] + dpp<XOR8>(y[i]), t2 = y[i + 4] + dpp<XOR8>(y[i + 4]); a[i] = (lane & 8) ? t2 : t1; }
    float z[2];
#pragma unroll
    for (int i = 0; i < 2; ++i) { auto r = __builtin_amdgcn_permlane16_swap(f2u(a[i]), f2u(a[i + 2]), false, false); z[i] = u2f(r[0]) + u2f(r[1]); }
    auto r = __builtin_amdgcn_permlane32_swap(f2u(z[0]), f2u(z[1]), false, false);
    return u2f(r[0]) + u2f(r[1]);
}
__device__ __forceinline__ float sum_hi3(float x) { x += dpp<XOR8>(x); return xrow_sum(x); }
__device__ __forceinline__ float max_hi3(float x) { x = fmaxf(x, dpp<XOR8>(x)); return xrow_max(x); }
__device__ __forceinline__ v4u ldw(const u16* p) {
    return __builtin_nontemporal_load((const v4u*)p);
}
#define BAR() do { asm volatile("s_waitcnt lgkmcnt(0)" ::: "memory"); __builtin_amdgcn_s_barrier(); asm volatile("" ::: "memory"); } while (0)
#define LD128(off) __builtin_amdgcn_raw_buffer_load_b128(rs, (off), 0, 16)
#define LD64(off) __builtin_amdgcn_raw_buffer_load_b64(rs, (off), 0, 16)
#define ST128(v, off) __builtin_amdgcn_raw_buffer_store_b128((v), rs, (off), 0, 16)

#define STAMP(i) do { } while (0)
#define STAMP_BEGIN() do { } while (0)
#define STAMP_END() do { } while (0)

__global__ void __launch_bounds__(NTHR) fused_decode(const u16* __restrict__ x, const u16* __restrict__ kc, const u16* __restrict__ vc,
                                                     const u16* __restrict__ wq, const u16* __restrict__ wo, const u16* __restrict__ w1,
                                                     const u16* __restrict__ w2, u16* __restrict__ out, unsigned char* ws) {
    __shared__ __attribute__((aligned(16))) unsigned char smem[LDS_BYTES];
    const int b = blockIdx.x, g = b & 7, s = b >> 3;
    const int tid = threadIdx.x, lane = tid & 63, wave = __builtin_amdgcn_readfirstlane(tid >> 6);
    const bool has1 = s < 28;
    const int cb = s >> 2, kq = s & 3;
    const int hh3 = g * 4 + (s >> 3), cg3 = s & 7;
    const __amdgpu_buffer_rsrc_t rs = __builtin_amdgcn_make_buffer_rsrc(ws, 0, WS_END, 0x00020000);
    const int c = lane & 15, quad = lane >> 4;
    const int r3 = lane >> 3, c3 = lane & 7;
    float* part = (float*)(smem + L_PART);
    asm volatile("" :: "s"(x), "s"(kc), "s"(vc), "s"(wq), "s"(wo), "s"(w1), "s"(w2), "s"(out), "s"(ws));
    STAMP_BEGIN();
    STAMP(0);
    const int prow = tid >> 3;
    const v4u xv = *(const v4u*)(x + lane * 8);
    const unsigned xq0 = x[kq * 128 + prow], xq1 = x[kq * 128 + 64 + prow], xh = x[g * 64 + prow];
    const u16* wqp = wq + (size_t)(kq * 128 + prow) * NQ + g * 512 + cb * 64 + c3 * 8;
    const v4u wq0 = ldw(wqp), wq1 = ldw(wqp + (size_t)64 * NQ);
    v4u w1v = {0u, 0u, 0u, 0u};
    if (has1) w1v = ldw(w1 + (size_t)(g * 64 + prow) * DFF + s * 64 + c3 * 8);
    unsigned G = 0;
    if (wave == 0) G = __builtin_amdgcn_raw_buffer_load_b32(rs, WS_G, 0, 16);
    const size_t kvbase = ((size_t)g * SK + (SK - WIN) + s * 128 + wave * 16) * DH;
    const u16* kp = kc + kvbase + (size_t)c * DH + quad * 8;
    const u16* vp = vc + kvbase + (size_t)(quad * 4) * DH + c * 8;
    const u16* wop = wo + (size_t)(hh3 * 128 + wave * 16 + r3) * D + cg3 * 64 + c3 * 8;
    const u16* w2p = w2 + (size_t)(hh3 * 56 + wave * 7 + (r3 < 7 ? r3 : 6)) * D + cg3 * 64 + c3 * 8;
    v4u kf[4], vf[4], wo0, wo1, w2v;
    if (tid < 16) *(v4u*)(smem + L_QS + 1024 + tid * 16) = (v4u){0u, 0u, 0u, 0u};
    __builtin_amdgcn_sched_barrier(0);
    float xe[8];
#pragma unroll
    for (int i = 0; i < 4; ++i) { xe[2 * i] = bflo(xv[i]); xe[2 * i + 1] = bfhi(xv[i]); }
    float sm = ((xe[0] + xe[1]) + (xe[2] + xe[3])) + ((xe[4] + xe[5]) + (xe[6] + xe[7]));
    const float mean = wave_sum(sm) * (1.f / 512.f);
    float sq = 0.f;
#pragma unroll
    for (int i = 0; i < 8; ++i) { const float d = xe[i] - mean; sq = fmaf(d, d, sq); }
    __builtin_amdgcn_sched_barrier(0);
#pragma unroll
    for (int kk = 0; kk < 4; ++kk) kf[kk] = ldw(kp + 32 * kk);
    __builtin_amdgcn_sched_barrier(0);
    const float var = wave_sum(sq) * (1.f / 512.f);
    const float den = rbf(sqrtf(rbf(rbf(var) + 1e-5f)));
    const float mb = rbf(mean);
    const float xn0 = rbf(rbf(bf2f(xq0) - mb) / den), xn1 = rbf(rbf(bf2f(xq1) - mb) / den), xnh = rbf(rbf(bf2f(xh) - mb) / den);
    {
        float a[8], h8[8];
#pragma unroll
        for (int i = 0; i < 4; ++i) {
            a[2 * i] = fmaf(xn1, bflo(wq1[i]), xn0 * bflo(wq0[i])); a[2 * i + 1] = fmaf(xn1, bfhi(wq1[i]), xn0 * bfhi(wq0[i]));
            h8[2 * i] = xnh * bflo(w1v[i]); h8[2 * i + 1] = xnh * bfhi(w1v[i]);
        }
        const float qa = tr_reduce8(a, lane), ha = tr_reduce8(h8, lane);
        const int col = 8 * c3 + ((lane >> 1) & 4) + ((lane >> 3) & 2) + (lane >> 5);
        part[wave * 128 + col] = qa; part[wave * 128 + 64 + col] = ha;
    }
    if (tid == 0) *(unsigned*)(smem + L_T) = G + 1u;
    STAMP(1);
    BAR();
    const unsigned T = __builtin_amdgcn_readfirstlane(*(const unsigned*)(smem + L_T));
    if (wave == 0) {
        v2f v = {0.f, 0.f};
#pragma unroll
        for (int w = 0; w < 8; ++w) v += *(const v2f*)(part + w * 128 + 2 * lane);
        const unsigned off = lane < 32 ? WS_QG + (unsigned)(((g * 8 + cb) * 4 + kq) * 64 + 2 * lane) * 8u : WS_HG + (unsigned)(g * 2048 + s * 64 + 2 * (lane - 32)) * 8u;
        if (lane < 32 || has1) ST128(((v4u){f2u(v[0]), T, f2u(v[1]), T}), off);
    }
    STAMP(2);
    {
        v4u qv[2]; bool okq[2] = {false, false};
        const unsigned qoff = WS_QG + (unsigned)(g * 8 + wave) * 2048u + lane * 16u;
#define QPOLL_ISSUE() v4u tq[2]; asm volatile("" ::: "memory"); _Pragma("unroll") for (int i = 0; i < 2; ++i) tq[i] = LD128(okq[i] ? OOB : qoff + i * 1024u); __builtin_amdgcn_sched_barrier(0)
#define QPOLL_CHECK() ({ bool ok = true; _Pragma("unroll") for (int i = 0; i < 2; ++i) { if (!okq[i]) { qv[i] = tq[i]; okq[i] = (tq[i][1] == T) & (tq[i][3] == T); } ok &= okq[i]; } __all(ok); })
        for (unsigned spins = 0;; ++spins) {
            QPOLL_ISSUE();
            if (QPOLL_CHECK() || spins > SPIN_CAP) break;
        }
        float q0 = u2f(qv[0][0]) + u2f(qv[1][0]), q1 = u2f(qv[0][2]) + u2f(qv[1][2]);
        auto r0 = __builtin_amdgcn_permlane32_swap(f2u(q0), f2u(q0), false, false); q0 = u2f(r0[0]) + u2f(r0[1]);
        auto r1 = __builtin_amdgcn_permlane32_swap(f2u(q1), f2u(q1), false, false); q1 = u2f(r1[0]) + u2f(r1[1]);
        if (lane < 32) *(unsigned*)(smem + L_QS + (wave * 64 + 2 * lane) * 2) = f2bf(q0) | (f2bf(q1) << 16);
    }
    STAMP(3);
    BAR();
#pragma unroll
    for (int jj = 0; jj < 4; ++jj) vf[jj] = ldw(vp + jj * DH);
    __builtin_amdgcn_sched_barrier(0);
    STAMP(4);
    v4f sc = {0.f, 0.f, 0.f, 0.f};
    {
        const unsigned char* qb = smem + L_QS + (c < 4 ? c * 256 + quad * 16 : 1024 + quad * 16);
#pragma unroll
        for (int kk = 0; kk < 4; ++kk)
            sc = __builtin_amdgcn_mfma_f32_16x16x32_bf16(__builtin_bit_cast(bf16x8, kf[kk]), __builtin_bit_cast(bf16x8, *(const v4u*)(qb + 64 * kk)), sc, 0, 0, 0);
    }
    float sv[4];
#pragma unroll
    for (int r = 0; r < 4; ++r) sv[r] = rbf(rbf(sc[r]) * 0.08838834764831845f);
    const float mw = xrow_max(fmaxf(fmaxf(sv[0], sv[1]), fmaxf(sv[2], sv[3])));
    float* ml = (float*)(smem + L_ML);
    {
        const float lw = xrow_sum((__expf(sv[0] - mw) + __expf(sv[1] - mw)) + (__expf(sv[2] - mw) + __expf(sv[3] - mw)));
        if (lane < 4) *(v2f*)(ml + (wave * 4 + lane) * 2) = (v2f){mw, lw};
    }
    STAMP(5);
    BAR();
    float M = -INFINITY;
#pragma unroll
    for (int w = 0; w < 8; ++w) M = fmaxf(M, ml[(w * 4 + (c & 3)) * 2]);
    const v2u pa = {f2bf(__expf(sv[0] - M)) | (f2bf(__expf(sv[1] - M)) << 16), f2bf(__expf(sv[2] - M)) | (f2bf(__expf(sv[3] - M)) << 16)};
    if (wave == 7) {
        float L = 0.f;
#pragma unroll
        for (int w = 0; w < 8; ++w) L = fmaf(__expf(ml[(w * 4 + (c & 3)) * 2] - M), ml[(w * 4 + (c & 3)) * 2 + 1], L);
        if (lane < 4) ST128(((v4u){f2u(M), T, f2u(L), T}), WS_ML + (unsigned)((g * 4 + lane) * 32 + s) * 16u);
    }
    STAMP(6);
    __builtin_amdgcn_sched_barrier(0);
    v4f o[8];
#pragma unroll
    for (int i = 0; i < 8; ++i) {
        const unsigned sel = (i & 1) ? 0x07060302u : 0x05040100u;
        const v2u bfr = {__builtin_amdgcn_perm(vf[1][i >> 1], vf[0][i >> 1], sel), __builtin_amdgcn_perm(vf[3][i >> 1], vf[2][i >> 1], sel)};
        o[i] = __builtin_amdgcn_mfma_f32_16x16x16bf16_1k(__builtin_bit_cast(v4s, pa), __builtin_bit_cast(v4s, bfr), (v4f){0.f, 0.f, 0.f, 0.f}, 0, 0, 0);
    }
    STAMP(7);
    float* ow = (float*)(smem + L_OW);
    if (quad == 0) {
#pragma unroll
        for (int h = 0; h < 4; ++h) {
            *(v4f*)(ow + (wave * 4 + h) * 128 + 8 * c) = (v4f){o[0][h], o[1][h], o[2][h], o[3][h]};
            *(v4f*)(ow + (wave * 4 + h) * 128 + 8 * c + 4) = (v4f){o[4][h], o[5][h], o[6][h], o[7][h]};
        }
    }
    BAR();
    STAMP(8);
    if (lane < 16) {
        const int h = wave >> 1, dq = (wave & 1) * 16 + lane;
        v4f a = {0.f, 0.f, 0.f, 0.f};
#pragma unroll
        for (int w = 0; w < 8; ++w) a += *(const v4f*)(ow + (w * 4 + h) * 128 + 4 * dq);
        ST128(((v4u){f2bf(a[0]) | (f2bf(a[1]) << 16), T, f2bf(a[2]) | (f2bf(a[3]) << 16), T}), WS_P2 + (unsigned)((((g * 4 + h) * 8 + (dq >> 2)) * 32 + s) * 4 + (dq & 3)) * 16u);
    }
    STAMP(9);
    __builtin_amdgcn_sched_barrier(0);
    wo0 = ldw(wop); wo1 = ldw(wop + 8 * D); w2v = ldw(w2p);
    __builtin_amdgcn_sched_barrier(0);
    float oa, ob, hv;
    {
        const unsigned pbase = WS_P2 + (unsigned)(hh3 * 8 + wave) * 2048u + lane * 16u;
        const unsigned mbase = WS_ML + (unsigned)(hh3 * 32 + (lane >> 2)) * 16u;
        const unsigned hoff = WS_HG + (unsigned)(r3 * 2048 + hh3 * 56 + wave * 7 + (c3 < 7 ? c3 : 6)) * 8u;
        v4u pv[2], mv[2]; v2u hg;
        bool okp[2] = {false, false}, okm[2] = {false, false}, okh = false;
        for (unsigned spins = 0;; ++spins) {
            asm volatile("" ::: "memory");
            v4u tp[2], tm[2];
#pragma unroll
            for (int i = 0; i < 2; ++i) { tm[i] = LD128(okm[i] ? OOB : mbase + i * 256u); }
            const v2u th = LD64(okh ? OOB : hoff);
#pragma unroll
            for (int i = 0; i < 2; ++i) { tp[i] = LD128(okp[i] ? OOB : pbase + i * 1024u); }
            bool ok = true;
#pragma unroll
            for (int i = 0; i < 2; ++i) {
                if (!okp[i]) { pv[i] = tp[i]; okp[i] = (tp[i][1] == T) & (tp[i][3] == T); }
                if (!okm[i]) { mv[i] = tm[i]; okm[i] = (tm[i][1] == T) & (tm[i][3] == T); }
                ok &= okp[i] & okm[i];
            }
            if (!okh) { hg = th; okh = th[1] == T; }
            ok &= okh;
            if (__all(ok) || spins > SPIN_CAP) break;
        }
        STAMP(10);
        float MM = fmaxf(u2f(mv[0][0]), u2f(mv[1][0]));
        MM = fmaxf(MM, dpp<ROR4>(MM)); MM = max_hi3(MM);
        const float e0 = __expf(u2f(mv[0][0]) - MM), e1 = __expf(u2f(mv[1][0]) - MM);
        float L = fmaf(e1, u2f(mv[1][2]), e0 * u2f(mv[0][2]));
        float o4[4] = {fmaf(e1, bflo(pv[1][0]), e0 * bflo(pv[0][0])), fmaf(e1, bfhi(pv[1][0]), e0 * bfhi(pv[0][0])),
                       fmaf(e1, bflo(pv[1][2]), e0 * bflo(pv[0][2])), fmaf(e1, bfhi(pv[1][2]), e0 * bfhi(pv[0][2]))};
        L += dpp<XOR8>(L); L += dpp<ROR4>(L); L = 1.f / xrow_sum(L);
#pragma unroll
        for (int k = 0; k < 4; ++k) { float t = o4[k]; t += dpp<XOR8>(t); t += dpp<ROR4>(t); o4[k] = rbf(xrow_sum(t) * L); }
        const float hs = fmaxf(rbf(sum_hi3(u2f(hg[0]))), 0.f);
        float* oq = (float*)(smem + L_OQ) + wave * 32;
        if (lane < 4) *(v4f*)(oq + 4 * lane) = (v4f){o4[0], o4[1], o4[2], o4[3]};
        if (lane < 8) oq[16 + lane] = lane < 7 ? hs : 0.f;
        asm volatile("s_waitcnt lgkmcnt(0)" ::: "memory");
        oa = oq[r3]; ob = oq[8 + r3]; hv = oq[16 + r3];
    }
    {
        float y[8];
#pragma unroll
        for (int i = 0; i < 4; ++i) {
            y[2 * i] = fmaf(hv, bflo(w2v[i]), fmaf(ob, bflo(wo1[i]), oa * bflo(wo0[i])));
            y[2 * i + 1] = fmaf(hv, bfhi(w2v[i]), fmaf(ob, bfhi(wo1[i]), oa * bfhi(wo0[i])));
        }
        const float ya = tr_reduce8(y, lane);
        const int col = 8 * c3 + ((lane >> 1) & 4) + ((lane >> 3) & 2) + (lane >> 5);
        part[wave * 128 + col] = ya;
    }
    STAMP(11);
    BAR();
    if (wave == 0) {
        if (lane < 32) {
            v2f y = {0.f, 0.f};
#pragma unroll
            for (int w = 0; w < 8; ++w) y += *(const v2f*)(part + w * 128 + 2 * lane);
            ST128(((v4u){f2u(y[0]), T, f2u(y[1]), T}), WS_P3 + (unsigned)(((cg3 * 4 + (lane >> 3)) * 32 + hh3) * 16 + 2 * (lane & 7)) * 8u);
        }
        STAMP(12);
    }
    if (wave == 1 && b < 32) {
        const unsigned xo = *(const unsigned*)(x + 16 * b + 2 * c3);
        v4u pv[4]; bool okp[4] = {false, false, false, false};
        for (unsigned spins = 0;; ++spins) {
            asm volatile("" ::: "memory");
            v4u tp[4]; bool ok = true;
#pragma unroll
            for (int i = 0; i < 4; ++i) tp[i] = LD128(okp[i] ? OOB : WS_P3 + (unsigned)b * 4096u + i * 1024u + lane * 16u);
#pragma unroll
            for (int i = 0; i < 4; ++i) { if (!okp[i]) { pv[i] = tp[i]; okp[i] = (tp[i][1] == T) & (tp[i][3] == T); } ok &= okp[i]; }
            if (__all(ok) || spins > SPIN_CAP) break;
        }
        const float y0 = sum_hi3((u2f(pv[0][0]) + u2f(pv[1][0])) + (u2f(pv[2][0]) + u2f(pv[3][0])));
        const float y1 = sum_hi3((u2f(pv[0][2]) + u2f(pv[1][2])) + (u2f(pv[2][2]) + u2f(pv[3][2])));
        if (lane < 8) *(unsigned*)(out + 16 * b + 2 * lane) = f2bf(bflo(xo) + y0) | (f2bf(bfhi(xo) + y1) << 16);
        if (b == 0 && lane == 0) __builtin_amdgcn_raw_buffer_store_b32(T, rs, WS_G, 0, 16);
        STAMP(13);
    }
    STAMP_END();
}

extern "C" void kernel_launch(void* const* d_in, const int* in_sizes, int n_in,
                              void* d_out, int out_size, void* d_ws, size_t ws_size,
                              hipStream_t stream) {
    fused_decode<<<NWG, NTHR, 0, stream>>>((const u16*)d_in[0], (const u16*)d_in[1], (const u16*)d_in[2], (const u16*)d_in[3], (const u16*)d_in[4],
                                           (const u16*)d_in[6], (const u16*)d_in[7], (u16*)d_out, (unsigned char*)d_ws);
}
```

```cpp
#include <hip/hip_runtime.h>
#include <hip/hip_bf16.h>
#include <stdint.h>

typedef unsigned short u16;
typedef unsigned v4u __attribute__((ext_vector_type(4)));
typedef unsigned v2u __attribute__((ext_vector_type(2)));
typedef float v4f __attribute__((ext_vector_type(4)));
typedef float v2f __attribute__((ext_vector_type(2)));
typedef short v4s __attribute__((ext_vector_type(4)));
typedef __bf16 bf16x8 __attribute__((ext_vector_type(8)));

constexpr int D = 512, SK = 65536, DH = 128, WIN = 4096, DFF = 1792, NQ = 4096;
constexpr int NWG = 256, NTHR = 512;
constexpr unsigned WS_G = 0;
constexpr unsigned WS_QG = 4096;
constexpr unsigned WS_HG = WS_QG + 16384 * 8;
constexpr unsigned WS_ML = WS_HG + 16384 * 8;
constexpr unsigned WS_P2 = WS_ML + 2048 * 8;
constexpr unsigned WS_P3 = WS_P2 + 65536 * 8;
constexpr unsigned WS_END = WS_P3 + 16384 * 8;
constexpr unsigned WS_STAMP = 4u << 20;
constexpr unsigned OOB = 0xfffffff0u;
constexpr int L_PART = 0;
constexpr int L_T = 4096;
constexpr int L_QS = 4352;
constexpr int L_ML = 5632;
constexpr int L_OQ = 6144;
constexpr int L_OW = 8192;
constexpr int LDS_BYTES = 84 * 1024;
constexpr unsigned SPIN_CAP = 1u << 18;

__device__ __forceinline__ float bf2f(unsigned h) { return __uint_as_float(h << 16); }
__device__ __forceinline__ float bflo(unsigned w) { return __uint_as_float(w << 16); }
__device__ __forceinline__ float bfhi(unsigned w) { return __uint_as_float(w & 0xffff0000u); }
__device__ __forceinline__ unsigned f2bf(float f) { __hip_bfloat16 h = __float2bfloat16(f); return (unsigned)__builtin_bit_cast(unsigned short, h); }
__device__ __forceinline__ float rbf(float f) { return bf2f(f2bf(f)); }
__device__ __forceinline__ float u2f(unsigned u) { return __uint_as_float(u); }
__device__ __forceinline__ unsigned f2u(float f) { return __float_as_uint(f); }
template <int CTRL> __device__ __forceinline__ float dpp(float x) {
    return __builtin_bit_cast(float, __builtin_amdgcn_mov_dpp(__builtin_bit_cast(int, x), CTRL, 0xf, 0xf, true));
}
constexpr int XOR1 = 0xB1, XOR2 = 0x4E, XOR7 = 0x141, XOR8 = 0x128, XOR15 = 0x140, ROR4 = 0x124;
__device__ __forceinline__ float xrow_sum(float x) {
    auto s = __builtin_amdgcn_permlane16_swap(f2u(x), f2u(x), false, false);
    x = u2f(s[0]) + u2f(s[1]);
    auto t = __builtin_amdgcn_permlane32_swap(f2u(x), f2u(x), false, false);
    return u2f(t[0]) + u2f(t[1]);
}
__device__ __forceinline__ float xrow_max(float x) {
    auto s = __builtin_amdgcn_permlane16_swap(f2u(x), f2u(x), false, false);
    x = fmaxf(u2f(s[0]), u2f(s[1]));
    auto t = __builtin_amdgcn_permlane32_swap(f2u(x), f2u(x), false, false);
    return fmaxf(u2f(t[0]), u2f(t[1]));
}
__device__ __forceinline__ float wave_sum(float x) {
    x += dpp<XOR1>(x); x += dpp<XOR2>(x); x += dpp<XOR7>(x); x += dpp<XOR15>(x);
    return xrow_sum(x);
}
__device__ __forceinline__ float tr_reduce8(const float (&y)[8], int lane) {
    float a[4];
#pragma unroll
    for (int i = 0; i < 4; ++i) { const float t1 = y[i] + dpp<XOR8>(y[i]), t2 = y[i + 4] + dpp<XOR8>(y[i + 4]); a[i] = (lane & 8) ? t2 : t1; }
    float z[2];
#pragma unroll
    for (int i = 0; i < 2; ++i) { auto r = __builtin_amdgcn_permlane16_swap(f2u(a[i]), f2u(a[i + 2]), false, false); z[i] = u2f(r[0]) + u2f(r[1]); }
    auto r = __builtin_amdgcn_permlane32_swap(f2u(z[0]), f2u(z[1]), false, false);
    return u2f(r[0]) + u2f(r[1]);
}
__device__ __forceinline__ float sum_hi3(float x) { x += dpp<XOR8>(x); return xrow_sum(x); }
__device__ __forceinline__ float sum_hi4(float x) { x += dpp<XOR8>(x); x += dpp<ROR4>(x); return xrow_sum(x); }
__device__ __forceinline__ float max_hi4(float x) { x = fmaxf(x, dpp<XOR8>(x)); x = fmaxf(x, dpp<ROR4>(x)); return xrow_max(x); }
__device__ __forceinline__ v4u ldw(const u16* p) { return __builtin_nontemporal_load((const v4u*)p); }
__device__ __forceinline__ v4u sel4(bool c, v4u a, v4u b) { return (v4u){c ? a[0] : b[0], c ? a[1] : b[1], c ? a[2] : b[2], c ? a[3] : b[3]}; }
#define BAR() do { asm volatile("s_waitcnt lgkmcnt(0)" ::: "memory"); __builtin_amdgcn_s_barrier(); asm volatile("" ::: "memory"); } while (0)
#define SCHED() __builtin_amdgcn_sched_barrier(0)
#define LD128(off) __builtin_amdgcn_raw_buffer_load_b128(rs, (off), 0, 16)
#define ST128(v, off) __builtin_amdgcn_raw_buffer_store_b128((v), rs, (off), 0, 16)
#define TAKE(dst, ok, tmp) do { dst = sel4(ok, dst, tmp); ok = ok | ((tmp[1] == T) & (tmp[3] == T)); } while (0)

#define STAMP(i) do { } while (0)
#define STAMP_BEGIN() do { } while (0)
#define STAMP_END() do { } while (0)

__global__ void __launch_bounds__(NTHR) fused_decode(const u16* __restrict__ x, const u16* __restrict__ kc, const u16* __restrict__ vc,
                                                     const u16* __restrict__ wq, const u16* __restrict__ wo, const u16* __restrict__ w1,
                                                     const u16* __restrict__ w2, u16* __restrict__ out, unsigned char* ws) {
    __shared__ __attribute__((aligned(16))) unsigned char smem[LDS_BYTES];
    const int b = blockIdx.x, g = b & 7, s = b >> 3;
    const int tid = threadIdx.x, lane = tid & 63, wave = __builtin_amdgcn_readfirstlane(tid >> 6);
    const bool has1 = s < 28;
    const int cb = s >> 2, kq = s & 3;
    const int hh3 = g * 4 + (s >> 3), cg3 = s & 7;
    const __amdgpu_buffer_rsrc_t rs = __builtin_amdgcn_make_buffer_rsrc(ws, 0, WS_END, 0x00020000);
    const int c = lane & 15, quad = lane >> 4;
    const int r3 = lane >> 3, c3 = lane & 7;
    const int tcol = 8 * c3 + ((lane >> 1) & 4) + ((lane >> 3) & 2) + (lane >> 5);
    float* part = (float*)(smem + L_PART);
    asm volatile("" :: "s"(x), "s"(kc), "s"(vc), "s"(wq), "s"(wo), "s"(w1), "s"(w2), "s"(out), "s"(ws));
    STAMP_BEGIN();
    STAMP(0);
    const int prow = tid >> 3;
    const v4u xv = *(const v4u*)(x + lane * 8);
    const unsigned xq0 = x[kq * 128 + prow], xq1 = x[kq * 128 + 64 + prow], xh = x[g * 64 + prow];
    const u16* wqp = wq + (size_t)(kq * 128 + prow) * NQ + g * 512 + cb * 64 + c3 * 8;
    const v4u wq0 = ldw(wqp), wq1 = ldw(wqp + (size_t)64 * NQ);
    v4u w1v = {0u, 0u, 0u, 0u};
    if (has1) w1v = ldw(w1 + (size_t)(g * 64 + prow) * DFF + s * 64 + c3 * 8);
    unsigned G = 0;
    if (wave == 0) G = __builtin_amdgcn_raw_buffer_load_b32(rs, WS_G, 0, 16);
    const size_t kvbase = ((size_t)g * SK + (SK - WIN) + s * 128 + wave * 16) * DH;
    const u16* kp = kc + kvbase + (size_t)c * DH + quad * 8;
    const u16* vp = vc + kvbase + (size_t)(quad * 4) * DH + c * 8;
    const u16* wop = wo + (size_t)(hh3 * 128 + wave * 16 + r3) * D + cg3 * 64 + c3 * 8;
    const u16* w2p = w2 + (size_t)(hh3 * 56 + wave * 7 + (r3 < 7 ? r3 : 6)) * D + cg3 * 64 + c3 * 8;
    unsigned o_qg = WS_QG + (unsigned)(g * 8 + wave) * 2048u + lane * 16u;
    unsigned o_p2st = WS_P2 + (unsigned)((((g * 4 + (wave >> 1)) * 8 + (wave & 1) * 4 + (c >> 2)) * 32 + s) * 4 + (c & 3)) * 16u;
    unsigned o_p2 = WS_P2 + (unsigned)(hh3 * 8 + wave) * 2048u + lane * 16u;
    unsigned o_ml = WS_ML + (unsigned)(hh3 * 32 + (lane >> 2)) * 16u;
    const int hrow = hh3 * 56 + wave * 7 + (c3 < 7 ? c3 : 6);
    unsigned o_hg = WS_HG + (unsigned)(r3 * 2048 + (hrow & ~1)) * 8u;
    unsigned o_p3st = WS_P3 + (unsigned)(((cg3 * 4 + ((lane >> 3) & 3)) * 32 + hh3) * 16 + 2 * c3) * 8u;
    if (tid < 16) *(v4u*)(smem + L_QS + 1024 + tid * 16) = (v4u){0u, 0u, 0u, 0u};
    SCHED();
    float xe[8];
#pragma unroll
    for (int i = 0; i < 4; ++i) { xe[2 * i] = bflo(xv[i]); xe[2 * i + 1] = bfhi(xv[i]); }
    const float sm = ((xe[0] + xe[1]) + (xe[2] + xe[3])) + ((xe[4] + xe[5]) + (xe[6] + xe[7]));
    const float mean = wave_sum(sm) * (1.f / 512.f);
    float sq = 0.f;
#pragma unroll
    for (int i = 0; i < 8; ++i) { const float d = xe[i] - mean; sq = fmaf(d, d, sq); }
    SCHED();
    v4u kf[4];
#pragma unroll
    for (int kk = 0; kk < 4; ++kk) kf[kk] = ldw(kp + 32 * kk);
    SCHED();
    asm volatile("" : "+v"(vp), "+v"(wop), "+v"(w2p), "+v"(o_qg), "+v"(o_p2st), "+v"(o_p2), "+v"(o_ml), "+v"(o_hg), "+v"(o_p3st));
    const float var = wave_sum(sq) * (1.f / 512.f);
    const float den = rbf(sqrtf(rbf(rbf(var) + 1e-5f)));
    const float mb = rbf(mean);
    const float xn0 = rbf(rbf(bf2f(xq0) - mb) / den), xn1 = rbf(rbf(bf2f(xq1) - mb) / den), xnh = rbf(rbf(bf2f(xh) - mb) / den);
    {
        float a[8], h8[8];
#pragma unroll
        for (int i = 0; i < 4; ++i) {
            a[2 * i] = fmaf(xn1, bflo(wq1[i]), xn0 * bflo(wq0[i])); a[2 * i + 1] = fmaf(xn1, bfhi(wq1[i]), xn0 * bfhi(wq0[i]));
            h8[2 * i] = xnh * bflo(w1v[i]); h8[2 * i + 1] = xnh * bfhi(w1v[i]);
        }
        part[wave * 128 + tcol] = tr_reduce8(a, lane); part[wave * 128 + 64 + tcol] = tr_reduce8(h8, lane);
    }
    if (tid == 0) *(unsigned*)(smem + L_T) = G + 0x9E3779B9u;
    STAMP(1);
    BAR();
    const unsigned T = __builtin_amdgcn_readfirstlane(*(const unsigned*)(smem + L_T));
    if (wave == 0) {
        v2f v = {0.f, 0.f};
#pragma unroll
        for (int w = 0; w < 8; ++w) v += *(const v2f*)(part + w * 128 + 2 * lane);
        const unsigned off = lane < 32 ? WS_QG + (unsigned)(((g * 8 + cb) * 4 + kq) * 64 + 2 * lane) * 8u : WS_HG + (unsigned)(g * 2048 + s * 64 + 2 * (lane - 32)) * 8u;
        if (lane < 32 || has1) ST128(((v4u){f2u(v[0]), T, f2u(v[1]), T}), off);
    }
    STAMP(2);
    {
        v4u q0v = {0u, 0u, 0u, 0u}, q1v = q0v; bool ok0 = false, ok1 = false;
        for (unsigned spins = 0;; ++spins) {
            asm volatile("" ::: "memory");
            const v4u t0 = LD128(ok0 ? OOB : o_qg), t1 = LD128(ok1 ? OOB : o_qg + 1024u);
            TAKE(q0v, ok0, t0); TAKE(q1v, ok1, t1);
            if (__all(ok0 & ok1) || spins > SPIN_CAP) break;
        }
        float q0 = u2f(q0v[0]) + u2f(q1v[0]), q1 = u2f(q0v[2]) + u2f(q1v[2]);
        auto r0 = __builtin_amdgcn_permlane32_swap(f2u(q0), f2u(q0), false, false); q0 = u2f(r0[0]) + u2f(r0[1]);
        auto r1 = __builtin_amdgcn_permlane32_swap(f2u(q1), f2u(q1), false, false); q1 = u2f(r1[0]) + u2f(r1[1]);
        if (lane < 32) *(unsigned*)(smem + L_QS + (wave * 64 + 2 * lane) * 2) = f2bf(q0) | (f2bf(q1) << 16);
    }
    STAMP(3);
    BAR();
    v4u vf[4];
#pragma unroll
    for (int jj = 0; jj < 4; ++jj) vf[jj] = ldw(vp + jj * DH);
    SCHED();
    STAMP(4);
    v4f sc = {0.f, 0.f, 0.f, 0.f};
    {
        const unsigned char* qb = smem + L_QS + (c < 4 ? c * 256 + quad * 16 : 1024 + quad * 16);
#pragma unroll
        for (int kk = 0; kk < 4; ++kk)
            sc = __builtin_amdgcn_mfma_f32_16x16x32_bf16(__builtin_bit_cast(bf16x8, kf[kk]), __builtin_bit_cast(bf16x8, *(const v4u*)(qb + 64 * kk)), sc, 0, 0, 0);
    }
    float sv[4];
#pragma unroll
    for (int r = 0; r < 4; ++r) sv[r] = rbf(rbf(sc[r]) * 0.08838834764831845f);
    const float mw = xrow_max(fmaxf(fmaxf(sv[0], sv[1]), fmaxf(sv[2], sv[3])));
    float* ml = (float*)(smem + L_ML);
    {
        const float lw = xrow_sum((__expf(sv[0] - mw) + __expf(sv[1] - mw)) + (__expf(sv[2] - mw) + __expf(sv[3] - mw)));
        if (lane < 4) *(v2f*)(ml + (wave * 4 + lane) * 2) = (v2f){mw, lw};
    }
    STAMP(5);
    BAR();
    float M = -INFINITY;
#pragma unroll
    for (int w = 0; w < 8; ++w) M = fmaxf(M, ml[(w * 4 + (c & 3)) * 2]);
    const v2u pa = {f2bf(__expf(sv[0] - M)) | (f2bf(__expf(sv[1] - M)) << 16), f2bf(__expf(sv[2] - M)) | (f2bf(__expf(sv[3] - M)) << 16)};
    if (wave == 0) {
        float L = 0.f;
#pragma unroll
        for (int w = 0; w < 8; ++w) L = fmaf(__expf(ml[(w * 4 + (c & 3)) * 2] - M), ml[(w * 4 + (c & 3)) * 2 + 1], L);
        if (lane < 4) ST128(((v4u){f2u(M), T, f2u(L), T}), WS_ML + (unsigned)((g * 4 + lane) * 32 + s) * 16u);
    }
    STAMP(6);
    SCHED();
    v4f o[8];
#pragma unroll
    for (int i = 0; i < 8; ++i) {
        const unsigned sel = (i & 1) ? 0x07060302u : 0x05040100u;
        const v2u bfr = {__builtin_amdgcn_perm(vf[1][i >> 1], vf[0][i >> 1], sel), __builtin_amdgcn_perm(vf[3][i >> 1], vf[2][i >> 1], sel)};
        o[i] = __builtin_amdgcn_mfma_f32_16x16x16bf16_1k(__builtin_bit_cast(v4s, pa), __builtin_bit_cast(v4s, bfr), (v4f){0.f, 0.f, 0.f, 0.f}, 0, 0, 0);
    }
    STAMP(7);
    float* ow = (float*)(smem + L_OW);
    if (quad == 0) {
#pragma unroll
        for (int h = 0; h < 4; ++h) {
            *(v4f*)(ow + (wave * 4 + h) * 128 + 8 * c) = (v4f){o[0][h], o[1][h], o[2][h], o[3][h]};
            *(v4f*)(ow + (wave * 4 + h) * 128 + 8 * c + 4) = (v4f){o[4][h], o[5][h], o[6][h], o[7][h]};
        }
    }
    BAR();
    STAMP(8);
    if (lane < 16) {
        const float* src = ow + (wave >> 1) * 128 + (wave & 1) * 64 + 4 * lane;
        v4f a = *(const v4f*)src;
#pragma unroll
        for (int w = 1; w < 8; ++w) a += *(const v4f*)(src + w * 512);
        ST128(((v4u){f2bf(a[0]) | (f2bf(a[1]) << 16), T, f2bf(a[2]) | (f2bf(a[3]) << 16), T}), o_p2st);
    }
    STAMP(9);
    SCHED();
    const v4u wo0 = ldw(wop), wo1 = ldw(wop + 8 * D), w2v = ldw(w2p);
    SCHED();
    float e0, e1;
    float* oq = (float*)(smem + L_OQ) + wave * 32;
    {
        v4u m0 = {0u, 0u, 0u, 0u}, m1 = m0, hg = m0; bool ok0 = false, ok1 = false, okh = false;
        for (unsigned spins = 0;; ++spins) {
            asm volatile("" ::: "memory");
            const v4u t0 = LD128(ok0 ? OOB : o_ml), t1 = LD128(ok1 ? OOB : o_ml + 256u), th = LD128(okh ? OOB : o_hg);
            TAKE(m0, ok0, t0); TAKE(m1, ok1, t1); TAKE(hg, okh, th);
            if (__all(ok0 & ok1 & okh) || spins > SPIN_CAP) break;
        }
        const float MM = max_hi4(fmaxf(u2f(m0[0]), u2f(m1[0])));
        e0 = __expf(u2f(m0[0]) - MM); e1 = __expf(u2f(m1[0]) - MM);
        const float inv = 1.f / sum_hi4(fmaf(e1, u2f(m1[2]), e0 * u2f(m0[2])));
        e0 *= inv; e1 *= inv;
        const float hs = fmaxf(rbf(sum_hi3(u2f((hrow & 1) ? hg[2] : hg[0]))), 0.f);
        if (lane < 8) oq[16 + lane] = lane < 7 ? hs : 0.f;
    }
    float wf[24];
#pragma unroll
    for (int i = 0; i < 4; ++i) { wf[2 * i] = bflo(wo0[i]); wf[2 * i + 1] = bfhi(wo0[i]); wf[8 + 2 * i] = bflo(wo1[i]); wf[9 + 2 * i] = bfhi(wo1[i]); wf[16 + 2 * i] = bflo(w2v[i]); wf[17 + 2 * i] = bfhi(w2v[i]); }
    SCHED();
    {
        v4u p0 = {0u, 0u, 0u, 0u}, p1 = p0; bool ok0 = false, ok1 = false;
        for (unsigned spins = 0;; ++spins) {
            asm volatile("" ::: "memory");
            const v4u t0 = LD128(ok0 ? OOB : o_p2), t1 = LD128(ok1 ? OOB : o_p2 + 1024u);
            TAKE(p0, ok0, t0); TAKE(p1, ok1, t1);
            if (__all(ok0 & ok1) || spins > SPIN_CAP) break;
        }
        STAMP(10);
        const v4f o4 = {rbf(sum_hi4(fmaf(e1, bflo(p1[0]), e0 * bflo(p0[0])))), rbf(sum_hi4(fmaf(e1, bfhi(p1[0]), e0 * bfhi(p0[0])))),
                        rbf(sum_hi4(fmaf(e1, bflo(p1[2]), e0 * bflo(p0[2])))), rbf(sum_hi4(fmaf(e1, bfhi(p1[2]), e0 * bfhi(p0[2]))))};
        if (lane < 4) *(v4f*)(oq + 4 * lane) = o4;
        asm volatile("s_waitcnt lgkmcnt(0)" ::: "memory");
    }
    {
        const float oa = oq[r3], ob = oq[8 + r3], hv = oq[16 + r3];
        float y[8];
#pragma unroll
        for (int i = 0; i < 8; ++i) y[i] = fmaf(hv, wf[16 + i], fmaf(ob, wf[8 + i], oa * wf[i]));
        part[wave * 128 + tcol] = tr_reduce8(y, lane);
    }
    STAMP(11);
    BAR();
    if (wave == 0) {
        if (lane < 32) {
            v2f y = *(const v2f*)(part + 2 * lane);
#pragma unroll
            for (int w = 1; w < 8; ++w) y += *(const v2f*)(part + w * 128 + 2 * lane);
            ST128(((v4u){f2u(y[0]), T, f2u(y[1]), T}), o_p3st);
        }
        STAMP(12);
    }
    if (wave == 1 && b < 32) {
        const unsigned xo = *(const unsigned*)(x + 16 * b + 2 * c3);
        const unsigned o_p3 = WS_P3 + (unsigned)b * 4096u + lane * 16u;
        v4u p0 = {0u, 0u, 0u, 0u}, p1 = p0, p2 = p0, p3 = p0; bool ok0 = false, ok1 = false, ok2 = false, ok3 = false;
        for (unsigned spins = 0;; ++spins) {
            asm volatile("" ::: "memory");
            const v4u t0 = LD128(ok0 ? OOB : o_p3), t1 = LD128(ok1 ? OOB : o_p3 + 1024u), t2 = LD128(ok2 ? OOB : o_p3 + 2048u), t3 = LD128(ok3 ? OOB : o_p3 + 3072u);
            TAKE(p0, ok0, t0); TAKE(p1, ok1, t1); TAKE(p2, ok2, t2); TAKE(p3, ok3, t3);
            if (__all(ok0 & ok1 & ok2 & ok3) || spins > SPIN_CAP) break;
        }
        const float y0 = sum_hi3((u2f(p0[0]) + u2f(p1[0])) + (u2f(p2[0]) + u2f(p3[0])));
        const float y1 = sum_hi3((u2f(p0[2]) + u2f(p1[2])) + (u2f(p2[2]) + u2f(p3[2])));
        if (lane < 8) *(unsigned*)(out + 16 * b + 2 * lane) = f2bf(bflo(xo) + y0) | (f2bf(bfhi(xo) + y1) << 16);
        if (b == 0 && lane == 0) __builtin_amdgcn_raw_buffer_store_b32(T, rs, WS_G, 0, 16);
        STAMP(13);
    }
    STAMP_END();
}

extern "C" void kernel_launch(void* const* d_in, const int* in_sizes, int n_in,
                              void* d_out, int out_size, void* d_ws, size_t ws_size,
                              hipStream_t stream) {
    fused_decode<<<NWG, NTHR, 0, stream>>>((const u16*)d_in[0], (const u16*)d_in[1], (const u16*)d_in[2], (const u16*)d_in[3], (const u16*)d_in[4],
                                           (const u16*)d_in[6], (const u16*)d_in[7], (u16*)d_out, (unsigned char*)d_ws);
}
```

```cpp
#include <hip/hip_runtime.h>
#include <hip/hip_bf16.h>
#include <stdint.h>

typedef unsigned short u16;
typedef unsigned v4u __attribute__((ext_vector_type(4)));
typedef unsigned v2u __attribute__((ext_vector_type(2)));
typedef float v4f __attribute__((ext_vector_type(4)));
typedef float v2f __attribute__((ext_vector_type(2)));
typedef short v4s __attribute__((ext_vector_type(4)));
typedef __bf16 bf16x8 __attribute__((ext_vector_type(8)));

#define V_EARLY 0
#define WO_POS 1
constexpr int D = 512, SK = 65536, DH = 128, WIN = 4096, DFF = 1792, NQ = 4096;
constexpr int NWG = 256, NTHR = 512;
constexpr unsigned WS_G = 0;
constexpr unsigned WS_QG = 4096;
constexpr unsigned WS_HG = WS_QG + 16384 * 8;
constexpr unsigned WS_ML = WS_HG + 16384 * 8;
constexpr unsigned WS_P2 = WS_ML + 2048 * 8;
constexpr unsigned WS_P3 = WS_P2 + 65536 * 8;
constexpr unsigned WS_END = WS_P3 + 16384 * 8;
constexpr unsigned WS_STAMP = 4u << 20;
constexpr unsigned OOB = 0xfffffff0u;
constexpr int L_PART = 0;
constexpr int L_T = 4096;
constexpr int L_QS = 4352;
constexpr int L_ML = 5632;
constexpr int L_OQ = 6144;
constexpr int L_OW = 8192;
#define LDS_KB 84
constexpr int LDS_BYTES = LDS_KB * 1024;
constexpr unsigned SPIN_CAP = 1u << 18;

__device__ __forceinline__ float bf2f(unsigned h) { return __uint_as_float(h << 16); }
__device__ __forceinline__ float bflo(unsigned w) { return __uint_as_float(w << 16); }
__device__ __forceinline__ float bfhi(unsigned w) { return __uint_as_float(w & 0xffff0000u); }
__device__ __forceinline__ unsigned f2bf(float f) { __hip_bfloat16 h = __float2bfloat16(f); return (unsigned)__builtin_bit_cast(unsigned short, h); }
__device__ __forceinline__ float rbf(float f) { return bf2f(f2bf(f)); }
__device__ __forceinline__ float u2f(unsigned u) { return __uint_as_float(u); }
__device__ __forceinline__ unsigned f2u(float f) { return __float_as_uint(f); }
template <int CTRL> __device__ __forceinline__ float dpp(float x) {
    return __builtin_bit_cast(float, __builtin_amdgcn_mov_dpp(__builtin_bit_cast(int, x), CTRL, 0xf, 0xf, true));
}
constexpr int XOR1 = 0xB1, XOR2 = 0x4E, XOR7 = 0x141, XOR8 = 0x128, XOR15 = 0x140, ROR4 = 0x124;
__device__ __forceinline__ float xrow_sum(float x) {
    auto s = __builtin_amdgcn_permlane16_swap(f2u(x), f2u(x), false, false);
    x = u2f(s[0]) + u2f(s[1]);
    auto t = __builtin_amdgcn_permlane32_swap(f2u(x), f2u(x), false, false);
    return u2f(t[0]) + u2f(t[1]);
}
__device__ __forceinline__ float xrow_max(float x) {
    auto s = __builtin_amdgcn_permlane16_swap(f2u(x), f2u(x), false, false);
    x = fmaxf(u2f(s[0]), u2f(s[1]));
    auto t = __builtin_amdgcn_permlane32_swap(f2u(x), f2u(x), false, false);
    return fmaxf(u2f(t[0]), u2f(t[1]));
}
__device__ __forceinline__ float wave_sum(float x) {
    x += dpp<XOR1>(x); x += dpp<XOR2>(x); x += dpp<XOR7>(x); x += dpp<XOR15>(x);
    return xrow_sum(x);
}
__device__ __forceinline__ float tr_reduce8(const float (&y)[8], int lane) {
    float a[4];
#pragma unroll
    for (int i = 0; i < 4; ++i) { const float t1 = y[i] + dpp<XOR8>(y[i]), t2 = y[i + 4] + dpp<XOR8>(y[i + 4]); a[i] = (lane & 8) ? t2 : t1; }
    float z[2];
#pragma unroll
    for (int i = 0; i < 2; ++i) { auto r = __builtin_amdgcn_permlane16_swap(f2u(a[i]), f2u(a[i + 2]), false, false); z[i] = u2f(r[0]) + u2f(r[1]); }
    auto r = __builtin_amdgcn_permlane32_swap(f2u(z[0]), f2u(z[1]), false, false);
    return u2f(r[0]) + u2f(r[1]);
}
__device__ __forceinline__ float sum_hi3(float x) { x += dpp<XOR8>(x); return xrow_sum(x); }
__device__ __forceinline__ float sum_hi4(float x) { x += dpp<XOR8>(x); x += dpp<ROR4>(x); return xrow_sum(x); }
__device__ __forceinline__ float max_hi4(float x) { x = fmaxf(x, dpp<XOR8>(x)); x = fmaxf(x, dpp<ROR4>(x)); return xrow_max(x); }
#define NT_W 1
#define NT_KV 1
__device__ __forceinline__ v4u ldw(const u16* p) { return NT_W ? __builtin_nontemporal_load((const v4u*)p) : *(const v4u*)p; }
__device__ __forceinline__ v4u ldkv(const u16* p) { return NT_KV ? __builtin_nontemporal_load((const v4u*)p) : *(const v4u*)p; }
__device__ __forceinline__ v4u sel4(bool c, v4u a, v4u b) { return (v4u){c ? a[0] : b[0], c ? a[1] : b[1], c ? a[2] : b[2], c ? a[3] : b[3]}; }
#define BAR() do { asm volatile("s_waitcnt lgkmcnt(0)" ::: "memory"); __builtin_amdgcn_s_barrier(); asm volatile("" ::: "memory"); } while (0)
#define SCHED() __builtin_amdgcn_sched_barrier(0)
#define LD128(off) __builtin_amdgcn_raw_buffer_load_b128(rs, (off), 0, 16)
#define ST128(v, off) __builtin_amdgcn_raw_buffer_store_b128((v), rs, (off), 0, 16)
#define TAKE(dst, ok, tmp) do { dst = sel4(ok, dst, tmp); ok = ok | ((tmp[1] == T) & (tmp[3] == T)); } while (0)

#define STAMP(i) do { } while (0)
#define STAMP_BEGIN() do { } while (0)
#define STAMP_END() do { } while (0)

__global__ void __launch_bounds__(NTHR) fused_decode(const u16* __restrict__ x, const u16* __restrict__ kc, const u16* __restrict__ vc,
                                                     const u16* __restrict__ wq, const u16* __restrict__ wo, const u16* __restrict__ w1,
                                                     const u16* __restrict__ w2, u16* __restrict__ out, unsigned char* ws) {
    __shared__ __attribute__((aligned(16))) unsigned char smem[LDS_BYTES];
    const int b = blockIdx.x, g = b & 7, s = b >> 3;
    const int tid = threadIdx.x, lane = tid & 63, wave = __builtin_amdgcn_readfirstlane(tid >> 6);
    const bool has1 = s < 28;
    const int cb = s >> 2, kq = s & 3;
    const int hh3 = g * 4 + (s >> 3), cg3 = s & 7;
    const __amdgpu_buffer_rsrc_t rs = __builtin_amdgcn_make_buffer_rsrc(ws, 0, WS_END, 0x00020000);
    const int c = lane & 15, quad = lane >> 4;
    const int r3 = lane >> 3, c3 = lane & 7;
    const int tcol = 8 * c3 + ((lane >> 1) & 4) + ((lane >> 3) & 2) + (lane >> 5);
    float* part = (float*)(smem + L_PART);
    asm volatile("" :: "s"(x), "s"(kc), "s"(vc), "s"(wq), "s"(wo), "s"(w1), "s"(w2), "s"(out), "s"(ws));
    STAMP_BEGIN();
    STAMP(0);
    const int prow = tid >> 3;
    const v4u xv = *(const v4u*)(x + lane * 8);
    const unsigned xq0 = x[kq * 128 + prow], xq1 = x[kq * 128 + 64 + prow], xh = x[g * 64 + prow];
    const u16* wqp = wq + (size_t)(kq * 128 + prow) * NQ + g * 512 + cb * 64 + c3 * 8;
    const v4u wq0 = ldw(wqp), wq1 = ldw(wqp + (size_t)64 * NQ);
    v4u w1v = {0u, 0u, 0u, 0u};
    if (has1) w1v = ldw(w1 + (size_t)(g * 64 + prow) * DFF + s * 64 + c3 * 8);
    unsigned G = 0;
    if (wave == 0) G = __builtin_amdgcn_raw_buffer_load_b32(rs, WS_G, 0, 16);
    const size_t kvbase = ((size_t)g * SK + (SK - WIN) + s * 128 + wave * 16) * DH;
    const u16* kp = kc + kvbase + (size_t)c * DH + quad * 8;
    const u16* vp = vc + kvbase + (size_t)(quad * 4) * DH + c * 8;
    const u16* wop = wo + (size_t)(hh3 * 128 + wave * 16 + r3) * D + cg3 * 64 + c3 * 8;
    const u16* w2p = w2 + (size_t)(hh3 * 56 + wave * 7 + (r3 < 7 ? r3 : 6)) * D + cg3 * 64 + c3 * 8;
    unsigned o_qg = WS_QG + (unsigned)(g * 8 + wave) * 2048u + lane * 16u;
    unsigned o_p2st = WS_P2 + (unsigned)((((g * 4 + (wave >> 1)) * 8 + (wave & 1) * 4 + (c >> 2)) * 32 + s) * 4 + (c & 3)) * 16u;
    unsigned o_p2 = WS_P2 + (unsigned)(hh3 * 8 + wave) * 2048u + lane * 16u;
    unsigned o_ml = WS_ML + (unsigned)(hh3 * 32 + (lane >> 2)) * 16u;
    const int hrow = hh3 * 56 + wave * 7 + (c3 < 7 ? c3 : 6);
    unsigned o_hg = WS_HG + (unsigned)(r3 * 2048 + (hrow & ~1)) * 8u;
    unsigned o_p3st = WS_P3 + (unsigned)(((cg3 * 4 + ((lane >> 3) & 3)) * 32 + hh3) * 16 + 2 * c3) * 8u;
    if (tid < 16) *(v4u*)(smem + L_QS + 1024 + tid * 16) = (v4u){0u, 0u, 0u, 0u};
    SCHED();
    float xe[8];
#pragma unroll
    for (int i = 0; i < 4; ++i) { xe[2 * i] = bflo(xv[i]); xe[2 * i + 1] = bfhi(xv[i]); }
    const float sm = ((xe[0] + xe[1]) + (xe[2] + xe[3])) + ((xe[4] + xe[5]) + (xe[6] + xe[7]));
    const float mean = wave_sum(sm) * (1.f / 512.f);
    float sq = 0.f;
#pragma unroll
    for (int i = 0; i < 8; ++i) { const float d = xe[i] - mean; sq = fmaf(d, d, sq); }
    SCHED();
    v4u kf[4];
#pragma unroll
    for (int kk = 0; kk < 4; ++kk) kf[kk] = ldkv(kp + 32 * kk);
    SCHED();
    asm volatile("" : "+v"(vp), "+v"(wop), "+v"(w2p), "+v"(o_qg), "+v"(o_p2st), "+v"(o_p2), "+v"(o_ml), "+v"(o_hg), "+v"(o_p3st));
    const float var = wave_sum(sq) * (1.f / 512.f);
    const float den = rbf(sqrtf(rbf(rbf(var) + 1e-5f)));
    const float mb = rbf(mean);
    const float xn0 = rbf(rbf(bf2f(xq0) - mb) / den), xn1 = rbf(rbf(bf2f(xq1) - mb) / den), xnh = rbf(rbf(bf2f(xh) - mb) / den);
    {
        float a[8], h8[8];
#pragma unroll
        for (int i = 0; i < 4; ++i) {
            a[2 * i] = fmaf(xn1, bflo(wq1[i]), xn0 * bflo(wq0[i])); a[2 * i + 1] = fmaf(xn1, bfhi(wq1[i]), xn0 * bfhi(wq0[i]));
            h8[2 * i] = xnh * bflo(w1v[i]); h8[2 * i + 1] = xnh * bfhi(w1v[i]);
        }
        part[wave * 128 + tcol] = tr_reduce8(a, lane); part[wave * 128 + 64 + tcol] = tr_reduce8(h8, lane);
    }
    if (tid == 0) *(unsigned*)(smem + L_T) = G + 0x9E3779B9u;
    STAMP(1);
    BAR();
    const unsigned T = __builtin_amdgcn_readfirstlane(*(const unsigned*)(smem + L_T));
    if (wave == 0) {
        v2f v = {0.f, 0.f};
#pragma unroll
        for (int w = 0; w < 8; ++w) v += *(const v2f*)(part + w * 128 + 2 * lane);
        const unsigned off = lane < 32 ? WS_QG + (unsigned)(((g * 8 + cb) * 4 + kq) * 64 + 2 * lane) * 8u : WS_HG + (unsigned)(g * 2048 + s * 64 + 2 * (lane - 32)) * 8u;
        if (lane < 32 || has1) ST128(((v4u){f2u(v[0]), T, f2u(v[1]), T}), off);
    }
    STAMP(2);
    v4u vf[4], wo0, wo1, w2v;
    SCHED();
#pragma unroll
    for (int jj = 0; jj < V_EARLY; ++jj) vf[jj] = ldkv(vp + jj * DH);
    SCHED();
    {
        v4u q0v = {0u, 0u, 0u, 0u}, q1v = q0v; bool ok0 = false, ok1 = false;
        for (unsigned spins = 0;; ++spins) {
            asm volatile("" ::: "memory");
            const v4u t0 = LD128(ok0 ? OOB : o_qg), t1 = LD128(ok1 ? OOB : o_qg + 1024u);
            TAKE(q0v, ok0, t0); TAKE(q1v, ok1, t1);
            if (__all(ok0 & ok1) || spins > SPIN_CAP) break;
        }
        float q0 = u2f(q0v[0]) + u2f(q1v[0]), q1 = u2f(q0v[2]) + u2f(q1v[2]);
        auto r0 = __builtin_amdgcn_permlane32_swap(f2u(q0), f2u(q0), false, false); q0 = u2f(r0[0]) + u2f(r0[1]);
        auto r1 = __builtin_amdgcn_permlane32_swap(f2u(q1), f2u(q1), false, false); q1 = u2f(r1[0]) + u2f(r1[1]);
        if (lane < 32) *(unsigned*)(smem + L_QS + (wave * 64 + 2 * lane) * 2) = f2bf(q0) | (f2bf(q1) << 16);
    }
    STAMP(3);
    BAR();
#pragma unroll
    for (int jj = V_EARLY; jj < 4; ++jj) vf[jj] = ldkv(vp + jj * DH);
    SCHED();
    STAMP(4);
    v4f sc = {0.f, 0.f, 0.f, 0.f};
    {
        const unsigned char* qb = smem + L_QS + (c < 4 ? c * 256 + quad * 16 : 1024 + quad * 16);
#pragma unroll
        for (int kk = 0; kk < 4; ++kk)
            sc = __builtin_amdgcn_mfma_f32_16x16x32_bf16(__builtin_bit_cast(bf16x8, kf[kk]), __builtin_bit_cast(bf16x8, *(const v4u*)(qb + 64 * kk)), sc, 0, 0, 0);
    }
    float sv[4];
#pragma unroll
    for (int r = 0; r < 4; ++r) sv[r] = rbf(rbf(sc[r]) * 0.08838834764831845f);
    const float mw = xrow_max(fmaxf(fmaxf(sv[0], sv[1]), fmaxf(sv[2], sv[3])));
    float* ml = (float*)(smem + L_ML);
    {
        const float lw = xrow_sum((__expf(sv[0] - mw) + __expf(sv[1] - mw)) + (__expf(sv[2] - mw) + __expf(sv[3] - mw)));
        if (lane < 4) *(v2f*)(ml + (wave * 4 + lane) * 2) = (v2f){mw, lw};
    }
    STAMP(5);
    BAR();
    float M = -INFINITY;
#pragma unroll
    for (int w = 0; w < 8; ++w) M = fmaxf(M, ml[(w * 4 + (c & 3)) * 2]);
    const v2u pa = {f2bf(__expf(sv[0] - M)) | (f2bf(__expf(sv[1] - M)) << 16), f2bf(__expf(sv[2] - M)) | (f2bf(__expf(sv[3] - M)) << 16)};
    if (wave == 0) {
        float L = 0.f;
#pragma unroll
        for (int w = 0; w < 8; ++w) L = fmaf(__expf(ml[(w * 4 + (c & 3)) * 2] - M), ml[(w * 4 + (c & 3)) * 2 + 1], L);
        if (lane < 4) ST128(((v4u){f2u(M), T, f2u(L), T}), WS_ML + (unsigned)((g * 4 + lane) * 32 + s) * 16u);
    }
    STAMP(6);
    SCHED();
    v4f o[8];
#pragma unroll
    for (int i = 0; i < 8; ++i) {
        const unsigned sel = (i & 1) ? 0x07060302u : 0x05040100u;
        const v2u bfr = {__builtin_amdgcn_perm(vf[1][i >> 1], vf[0][i >> 1], sel), __builtin_amdgcn_perm(vf[3][i >> 1], vf[2][i >> 1], sel)};
        o[i] = __builtin_amdgcn_mfma_f32_16x16x16bf16_1k(__builtin_bit_cast(v4s, pa), __builtin_bit_cast(v4s, bfr), (v4f){0.f, 0.f, 0.f, 0.f}, 0, 0, 0);
    }
    STAMP(7);
    float* ow = (float*)(smem + L_OW);
    if (quad == 0) {
#pragma unroll
        for (int h = 0; h < 4; ++h) {
            *(v4f*)(ow + (wave * 4 + h) * 128 + 8 * c) = (v4f){o[0][h], o[1][h], o[2][h], o[3][h]};
            *(v4f*)(ow + (wave * 4 + h) * 128 + 8 * c + 4) = (v4f){o[4][h], o[5][h], o[6][h], o[7][h]};
        }
    }
    BAR();
    STAMP(8);
    if (lane < 16) {
        const float* src = ow + (wave >> 1) * 128 + (wave & 1) * 64 + 4 * lane;
        v4f a = *(const v4f*)src;
#pragma unroll
        for (int w = 1; w < 8; ++w) a += *(const v4f*)(src + w * 512);
        ST128(((v4u){f2bf(a[0]) | (f2bf(a[1]) << 16), T, f2bf(a[2]) | (f2bf(a[3]) << 16), T}), o_p2st);
    }
    STAMP(9);
    SCHED();
    wo0 = ldw(wop); wo1 = ldw(wop + 8 * D); w2v = ldw(w2p);
    SCHED();
    float e0, e1;
    float* oq = (float*)(smem + L_OQ) + wave * 32;
    {
        v4u m0 = {0u, 0u, 0u, 0u}, m1 = m0, hg = m0; bool ok0 = false, ok1 = false, okh = false;
        for (unsigned spins = 0;; ++spins) {
            asm volatile("" ::: "memory");
            const v4u t0 = LD128(ok0 ? OOB : o_ml), t1 = LD128(ok1 ? OOB : o_ml + 256u), th = LD128(okh ? OOB : o_hg);
            TAKE(m0, ok0, t0); TAKE(m1, ok1, t1); TAKE(hg, okh, th);
            if (__all(ok0 & ok1 & okh) || spins > SPIN_CAP) break;
        }
        STAMP(14);
        const float MM = max_hi4(fmaxf(u2f(m0[0]), u2f(m1[0])));
        e0 = __expf(u2f(m0[0]) - MM); e1 = __expf(u2f(m1[0]) - MM);
        const float inv = 1.f / sum_hi4(fmaf(e1, u2f(m1[2]), e0 * u2f(m0[2])));
        e0 *= inv; e1 *= inv;
        const float hs = fmaxf(rbf(sum_hi3(u2f((hrow & 1) ? hg[2] : hg[0]))), 0.f);
        if (lane < 8) oq[16 + lane] = lane < 7 ? hs : 0.f;
    }
    float wf[24];
#pragma unroll
    for (int i = 0; i < 4; ++i) { wf[2 * i] = bflo(wo0[i]); wf[2 * i + 1] = bfhi(wo0[i]); wf[8 + 2 * i] = bflo(wo1[i]); wf[9 + 2 * i] = bfhi(wo1[i]); wf[16 + 2 * i] = bflo(w2v[i]); wf[17 + 2 * i] = bfhi(w2v[i]); }
    SCHED();
    {
        v4u p0 = {0u, 0u, 0u, 0u}, p1 = p0; bool ok0 = false, ok1 = false;
        for (unsigned spins = 0;; ++spins) {
            asm volatile("" ::: "memory");
            const v4u t0 = LD128(ok0 ? OOB : o_p2), t1 = LD128(ok1 ? OOB : o_p2 + 1024u);
            TAKE(p0, ok0, t0); TAKE(p1, ok1, t1);
            if (__all(ok0 & ok1) || spins > SPIN_CAP) break;
        }
        STAMP(10);
        const v4f o4 = {rbf(sum_hi4(fmaf(e1, bflo(p1[0]), e0 * bflo(p0[0])))), rbf(sum_hi4(fmaf(e1, bfhi(p1[0]), e0 * bfhi(p0[0])))),
                        rbf(sum_hi4(fmaf(e1, bflo(p1[2]), e0 * bflo(p0[2])))), rbf(sum_hi4(fmaf(e1, bfhi(p1[2]), e0 * bfhi(p0[2]))))};
        if (lane < 4) *(v4f*)(oq + 4 * lane) = o4;
        asm volatile("s_waitcnt lgkmcnt(0)" ::: "memory");
    }
    {
        const float oa = oq[r3], ob = oq[8 + r3], hv = oq[16 + r3];
        float y[8];
#pragma unroll
        for (int i = 0; i < 8; ++i) y[i] = fmaf(hv, wf[16 + i], fmaf(ob, wf[8 + i], oa * wf[i]));
        part[wave * 128 + tcol] = tr_reduce8(y, lane);
    }
    STAMP(11);
    BAR();
    if (wave == 0) {
        if (lane < 32) {
            v2f y = *(const v2f*)(part + 2 * lane);
#pragma unroll
            for (int w = 1; w < 8; ++w) y += *(const v2f*)(part + w * 128 + 2 * lane);
            ST128(((v4u){f2u(y[0]), T, f2u(y[1]), T}), o_p3st);
        }
        STAMP(12);
    }
    if (wave == 1 && b < 32) {
        const unsigned xo = *(const unsigned*)(x + 16 * b + 2 * c3);
        const unsigned o_p3 = WS_P3 + (unsigned)b * 4096u + lane * 16u;
        v4u p0 = {0u, 0u, 0u, 0u}, p1 = p0, p2 = p0, p3 = p0; bool ok0 = false, ok1 = false, ok2 = false, ok3 = false;
        for (unsigned spins = 0;; ++spins) {
            asm volatile("" ::: "memory");
            const v4u t0 = LD128(ok0 ? OOB : o_p3), t1 = LD128(ok1 ? OOB : o_p3 + 1024u), t2 = LD128(ok2 ? OOB : o_p3 + 2048u), t3 = LD128(ok3 ? OOB : o_p3 + 3072u);
            TAKE(p0, ok0, t0); TAKE(p1, ok1, t1); TAKE(p2, ok2, t2); TAKE(p3, ok3, t3);
            if (__all(ok0 & ok1 & ok2 & ok3) || spins > SPIN_CAP) break;
        }
        const float y0 = sum_hi3((u2f(p0[0]) + u2f(p1[0])) + (u2f(p2[0]) + u2f(p3[0])));
        const float y1 = sum_hi3((u2f(p0[2]) + u2f(p1[2])) + (u2f(p2[2]) + u2f(p3[2])));
        if (lane < 8) *(unsigned*)(out + 16 * b + 2 * lane) = f2bf(bflo(xo) + y0) | (f2bf(bfhi(xo) + y1) << 16);
        if (b == 0 && lane == 0) __builtin_amdgcn_raw_buffer_store_b32(T, rs, WS_G, 0, 16);
        STAMP(13);
    }
    STAMP_END();
}

extern "C" void kernel_launch(void* const* d_in, const int* in_sizes, int n_in,
                              void* d_out, int out_size, void* d_ws, size_t ws_size,
                              hipStream_t stream) {
    fused_decode<<<NWG, NTHR, 0, stream>>>((const u16*)d_in[0], (const u16*)d_in[1], (const u16*)d_in[2], (const u16*)d_in[3], (const u16*)d_in[4],
                                           (const u16*)d_in[6], (const u16*)d_in[7], (u16*)d_out, (unsigned char*)d_ws);
}
```

```cpp
#include <hip/hip_runtime.h>
#include <hip/hip_bf16.h>
#include <stdint.h>

typedef unsigned short u16;
typedef unsigned v4u __attribute__((ext_vector_type(4)));
typedef unsigned v2u __attribute__((ext_vector_type(2)));
typedef float v4f __attribute__((ext_vector_type(4)));
typedef float v2f __attribute__((ext_vector_type(2)));
typedef short v4s __attribute__((ext_vector_type(4)));
typedef __bf16 bf16x8 __attribute__((ext_vector_type(8)));

constexpr int D = 512, SK = 65536, DH = 128, WIN = 4096, DFF = 1792, NQ = 4096;
constexpr int NWG = 256, NTHR = 512;
constexpr unsigned WS_G = 0;
constexpr unsigned WS_QG = 4096;
constexpr unsigned WS_HG = WS_QG + 16384 * 8;
constexpr unsigned WS_ML = WS_HG + 16384 * 8;
constexpr unsigned WS_P2 = WS_ML + 2048 * 8;
constexpr unsigned WS_P3 = WS_P2 + 65536 * 8;
constexpr unsigned WS_END = WS_P3 + 16384 * 8;
constexpr unsigned OOB = 0xfffffff0u;
constexpr int L_PART = 0;
constexpr int L_T = 4096;
constexpr int L_QS = 4352;
constexpr int L_ML = 5632;
constexpr int L_OQ = 6144;
constexpr int L_OW = 8192;
constexpr int LDS_BYTES = 84 * 1024;
constexpr unsigned SPIN_CAP = 1u << 18;

__device__ __forceinline__ float bf2f(unsigned h) { return __uint_as_float(h << 16); }
__device__ __forceinline__ float bflo(unsigned w) { return __uint_as_float(w << 16); }
__device__ __forceinline__ float bfhi(unsigned w) { return __uint_as_float(w & 0xffff0000u); }
__device__ __forceinline__ unsigned f2bf(float f) { __hip_bfloat16 h = __float2bfloat16(f); return (unsigned)__builtin_bit_cast(unsigned short, h); }
__device__ __forceinline__ float rbf(float f) { return bf2f(f2bf(f)); }
__device__ __forceinline__ float u2f(unsigned u) { return __uint_as_float(u); }
__device__ __forceinline__ unsigned f2u(float f) { return __float_as_uint(f); }
template <int CTRL> __device__ __forceinline__ float dpp(float x) {
    return __builtin_bit_cast(float, __builtin_amdgcn_mov_dpp(__builtin_bit_cast(int, x), CTRL, 0xf, 0xf, true));
}
constexpr int XOR1 = 0xB1, XOR2 = 0x4E, XOR7 = 0x141, XOR8 = 0x128, XOR15 = 0x140, ROR4 = 0x124;
__device__ __forceinline__ float xrow_sum(float x) {
    auto s = __builtin_amdgcn_permlane16_swap(f2u(x), f2u(x), false, false);
    x = u2f(s[0]) + u2f(s[1]);
    auto t = __builtin_amdgcn_permlane32_swap(f2u(x), f2u(x), false, false);
    return u2f(t[0]) + u2f(t[1]);
}
__device__ __forceinline__ float xrow_max(float x) {
    auto s = __builtin_amdgcn_permlane16_swap(f2u(x), f2u(x), false, false);
    x = fmaxf(u2f(s[0]), u2f(s[1]));
    auto t = __builtin_amdgcn_permlane32_swap(f2u(x), f2u(x), false, false);
    return fmaxf(u2f(t[0]), u2f(t[1]));
}
__device__ __forceinline__ float wave_sum(float x) {
    x += dpp<XOR1>(x); x += dpp<XOR2>(x); x += dpp<XOR7>(x); x += dpp<XOR15>(x);
    return xrow_sum(x);
}
__device__ __forceinline__ float tr_reduce8(const float (&y)[8], int lane) {
    float a[4];
#pragma unroll
    for (int i = 0; i < 4; ++i) { const float t1 = y[i] + dpp<XOR8>(y[i]), t2 = y[i + 4] + dpp<XOR8>(y[i + 4]); a[i] = (lane & 8) ? t2 : t1; }
    float z[2];
#pragma unroll
    for (int i = 0; i < 2; ++i) { auto r = __builtin_amdgcn_permlane16_swap(f2u(a[i]), f2u(a[i + 2]), false, false); z[i] = u2f(r[0]) + u2f(r[1]); }
    auto r = __builtin_amdgcn_permlane32_swap(f2u(z[0]), f2u(z[1]), false, false);
    return u2f(r[0]) + u2f(r[1]);
}
__device__ __forceinline__ float sum_hi3(float x) { x += dpp<XOR8>(x); return xrow_sum(x); }
__device__ __forceinline__ float sum_hi4(float x) { x += dpp<XOR8>(x); x += dpp<ROR4>(x); return xrow_sum(x); }
__device__ __forceinline__ float max_hi4(float x) { x = fmaxf(x, dpp<XOR8>(x)); x = fmaxf(x, dpp<ROR4>(x)); return xrow_max(x); }
__device__ __forceinline__ v4u ldw(const u16* p) { return __builtin_nontemporal_load((const v4u*)p); }
__device__ __forceinline__ v4u sel4(bool c, v4u a, v4u b) { return (v4u){c ? a[0] : b[0], c ? a[1] : b[1], c ? a[2] : b[2], c ? a[3] : b[3]}; }
#define BAR() do { asm volatile("s_waitcnt lgkmcnt(0)" ::: "memory"); __builtin_amdgcn_s_barrier(); asm volatile("" ::: "memory"); } while (0)
#define SCHED() __builtin_amdgcn_sched_barrier(0)
#define LD128(off) __builtin_amdgcn_raw_buffer_load_b128(rs, (off), 0, 16)
#define ST128(v, off) __builtin_amdgcn_raw_buffer_store_b128((v), rs, (off), 0, 16)
#define TAKE(dst, ok, tmp) do { dst = sel4(ok, dst, tmp); ok = ok | ((tmp[1] == T) & (tmp[3] == T)); } while (0)

__global__ void __launch_bounds__(NTHR, 2) fused_decode(const u16* __restrict__ x, const u16* __restrict__ kc, const u16* __restrict__ vc,
                                                     const u16* __restrict__ wq, const u16* __restrict__ wo, const u16* __restrict__ w1,
                                                     const u16* __restrict__ w2, u16* __restrict__ out, unsigned char* ws) {
    __shared__ __attribute__((aligned(16))) unsigned char smem[LDS_BYTES];
    const int b = blockIdx.x, g = b & 7, s = b >> 3;
    const int tid = threadIdx.x, lane = tid & 63, wave = __builtin_amdgcn_readfirstlane(tid >> 6);
    const bool has1 = s < 28;
    const int cb = s >> 2, kq = s & 3;
    const int hh3 = g * 4 + (s >> 3), cg3 = s & 7;
    const __amdgpu_buffer_rsrc_t rs = __builtin_amdgcn_make_buffer_rsrc(ws, 0, WS_END, 0x00020000);
    const int c = lane & 15, quad = lane >> 4;
    const int r3 = lane >> 3, c3 = lane & 7;
    const int tcol = 8 * c3 + ((lane >> 1) & 4) + ((lane >> 3) & 2) + (lane >> 5);
    float* part = (float*)(smem + L_PART);
    asm volatile("" :: "s"(x), "s"(kc), "s"(vc), "s"(wq), "s"(wo), "s"(w1), "s"(w2), "s"(out), "s"(ws));
    const int prow = tid >> 3;
    const v4u xv = *(const v4u*)(x + lane * 8);
    const unsigned xq0 = x[kq * 128 + prow], xq1 = x[kq * 128 + 64 + prow], xh = x[g * 64 + prow];
    const u16* wqp = wq + (size_t)(kq * 128 + prow) * NQ + g * 512 + cb * 64 + c3 * 8;
    const v4u wq0 = ldw(wqp), wq1 = ldw(wqp + (size_t)64 * NQ);
    v4u w1v = {0u, 0u, 0u, 0u};
    if (has1) w1v = ldw(w1 + (size_t)(g * 64 + prow) * DFF + s * 64 + c3 * 8);
    unsigned G = 0;
    if (wave == 0) G = __builtin_amdgcn_raw_buffer_load_b32(rs, WS_G, 0, 16);
    const size_t kvbase = ((size_t)g * SK + (SK - WIN) + s * 128 + wave * 16) * DH;
    const u16* kp = kc + kvbase + (size_t)c * DH + quad * 8;
    const u16* vp = vc + kvbase + (size_t)(quad * 4) * DH + c * 8;
    const u16* wop = wo + (size_t)(hh3 * 128 + wave * 16 + r3) * D + cg3 * 64 + c3 * 8;
    const u16* w2p = w2 + (size_t)(hh3 * 56 + wave * 7 + (r3 < 7 ? r3 : 6)) * D + cg3 * 64 + c3 * 8;
    unsigned o_qg = WS_QG + (unsigned)(g * 8 + wave) * 2048u + lane * 16u;
    unsigned o_p2st = WS_P2 + (unsigned)((((g * 4 + (wave >> 1)) * 8 + (wave & 1) * 4 + (c >> 2)) * 32 + s) * 4 + (c & 3)) * 16u;
    unsigned o_p2 = WS_P2 + (unsigned)(hh3 * 8 + wave) * 2048u + lane * 16u;
    unsigned o_ml = WS_ML + (unsigned)(hh3 * 32 + (lane >> 2)) * 16u;
    const int hrow = hh3 * 56 + wave * 7 + (c3 < 7 ? c3 : 6);
    unsigned o_hg = WS_HG + (unsigned)(r3 * 2048 + (hrow & ~1)) * 8u;
    unsigned o_p3st = WS_P3 + (unsigned)(((cg3 * 4 + ((lane >> 3) & 3)) * 32 + hh3) * 16 + 2 * c3) * 8u;
    if (tid < 16) *(v4u*)(smem + L_QS + 1024 + tid * 16) = (v4u){0u, 0u, 0u, 0u};
    SCHED();
    float xe[8];
#pragma unroll
    for (int i = 0; i < 4; ++i) { xe[2 * i] = bflo(xv[i]); xe[2 * i + 1] = bfhi(xv[i]); }
    const float sm = ((xe[0] + xe[1]) + (xe[2] + xe[3])) + ((xe[4] + xe[5]) + (xe[6] + xe[7]));
    const float mean = wave_sum(sm) * (1.f / 512.f);
    float sq = 0.f;
#pragma unroll
    for (int i = 0; i < 8; ++i) { const float d = xe[i] - mean; sq = fmaf(d, d, sq); }
    SCHED();
    v4u kf[4];
#pragma unroll
    for (int kk = 0; kk < 4; ++kk) kf[kk] = ldw(kp + 32 * kk);
    SCHED();
    asm volatile("" : "+v"(vp), "+v"(wop), "+v"(w2p), "+v"(o_qg), "+v"(o_p2st), "+v"(o_p2), "+v"(o_ml), "+v"(o_hg), "+v"(o_p3st));
    const float var = wave_sum(sq) * (1.f / 512.f);
    const float den = rbf(sqrtf(rbf(rbf(var) + 1e-5f)));
    const float mb = rbf(mean);
    const float xn0 = rbf(rbf(bf2f(xq0) - mb) / den), xn1 = rbf(rbf(bf2f(xq1) - mb) / den), xnh = rbf(rbf(bf2f(xh) - mb) / den);
    {
        float a[8], h8[8];
#pragma unroll
        for (int i = 0; i < 4; ++i) {
            a[2 * i] = fmaf(xn1, bflo(wq1[i]), xn0 * bflo(wq0[i])); a[2 * i + 1] = fmaf(xn1, bfhi(wq1[i]), xn0 * bfhi(wq0[i]));
            h8[2 * i] = xnh * bflo(w1v[i]); h8[2 * i + 1] = xnh * bfhi(w1v[i]);
        }
        part[wave * 128 + tcol] = tr_reduce8(a, lane); part[wave * 128 + 64 + tcol] = tr_reduce8(h8, lane);
    }
    if (tid == 0) *(unsigned*)(smem + L_T) = G + 0x9E3779B9u;
    BAR();
    const unsigned T = __builtin_amdgcn_readfirstlane(*(const unsigned*)(smem + L_T));
    if (wave == 0) {
        v2f v = {0.f, 0.f};
#pragma unroll
        for (int w = 0; w < 8; ++w) v += *(const v2f*)(part + w * 128 + 2 * lane);
        const unsigned off = lane < 32 ? WS_QG + (unsigned)(((g * 8 + cb) * 4 + kq) * 64 + 2 * lane) * 8u : WS_HG + (unsigned)(g * 2048 + s * 64 + 2 * (lane - 32)) * 8u;
        if (lane < 32 || has1) ST128(((v4u){f2u(v[0]), T, f2u(v[1]), T}), off);
    }
    v4u vf[4], wo0, wo1, w2v;
    {
        v4u q0v = {0u, 0u, 0u, 0u}, q1v = q0v; bool ok0 = false, ok1 = false;
        __builtin_amdgcn_s_sleep(24);
        for (unsigned spins = 0;; ++spins) {
            asm volatile("" ::: "memory");
            const v4u t0 = LD128(ok0 ? OOB : o_qg), t1 = LD128(ok1 ? OOB : o_qg + 1024u);
            TAKE(q0v, ok0, t0); TAKE(q1v, ok1, t1);
            if (__all(ok0 & ok1) || spins > SPIN_CAP) break;
        }
        float q0 = u2f(q0v[0]) + u2f(q1v[0]), q1 = u2f(q0v[2]) + u2f(q1v[2]);
        auto r0 = __builtin_amdgcn_permlane32_swap(f2u(q0), f2u(q0), false, false); q0 = u2f(r0[0]) + u2f(r0[1]);
        auto r1 = __builtin_amdgcn_permlane32_swap(f2u(q1), f2u(q1), false, false); q1 = u2f(r1[0]) + u2f(r1[1]);
        if (lane < 32) *(unsigned*)(smem + L_QS + (wave * 64 + 2 * lane) * 2) = f2bf(q0) | (f2bf(q1) << 16);
    }
    BAR();
#pragma unroll
    for (int jj = 0; jj < 4; ++jj) vf[jj] = ldw(vp + jj * DH);
    SCHED();
    v4f sc = {0.f, 0.f, 0.f, 0.f};
    {
        const unsigned char* qb = smem + L_QS + (c < 4 ? c * 256 + quad * 16 : 1024 + quad * 16);
#pragma unroll
        for (int kk = 0; kk < 4; ++kk)
            sc = __builtin_amdgcn_mfma_f32_16x16x32_bf16(__builtin_bit_cast(bf16x8, kf[kk]), __builtin_bit_cast(bf16x8, *(const v4u*)(qb + 64 * kk)), sc, 0, 0, 0);
    }
    float sv[4];
#pragma unroll
    for (int r = 0; r < 4; ++r) sv[r] = rbf(rbf(sc[r]) * 0.08838834764831845f);
    const float mw = xrow_max(fmaxf(fmaxf(sv[0], sv[1]), fmaxf(sv[2], sv[3])));
    float* ml = (float*)(smem + L_ML);
    {
        const float lw = xrow_sum((__expf(sv[0] - mw) + __expf(sv[1] - mw)) + (__expf(sv[2] - mw) + __expf(sv[3] - mw)));
        if (lane < 4) *(v2f*)(ml + (wave * 4 + lane) * 2) = (v2f){mw, lw};
    }
    BAR();
    float M = -INFINITY;
#pragma unroll
    for (int w = 0; w < 8; ++w) M = fmaxf(M, ml[(w * 4 + (c & 3)) * 2]);
    const v2u pa = {f2bf(__expf(sv[0] - M)) | (f2bf(__expf(sv[1] - M)) << 16), f2bf(__expf(sv[2] - M)) | (f2bf(__expf(sv[3] - M)) << 16)};
    if (wave == 0) {
        float L = 0.f;
#pragma unroll
        for (int w = 0; w < 8; ++w) L = fmaf(__expf(ml[(w * 4 + (c & 3)) * 2] - M), ml[(w * 4 + (c & 3)) * 2 + 1], L);
        if (lane < 4) ST128(((v4u){f2u(M), T, f2u(L), T}), WS_ML + (unsigned)((g * 4 + lane) * 32 + s) * 16u);
    }
    SCHED();
    v4f o[8];
#pragma unroll
    for (int i = 0; i < 8; ++i) {
        const unsigned sel = (i & 1) ? 0x07060302u : 0x05040100u;
        const v2u bfr = {__builtin_amdgcn_perm(vf[1][i >> 1], vf[0][i >> 1], sel), __builtin_amdgcn_perm(vf[3][i >> 1], vf[2][i >> 1], sel)};
        o[i] = __builtin_amdgcn_mfma_f32_16x16x16bf16_1k(__builtin_bit_cast(v4s, pa), __builtin_bit_cast(v4s, bfr), (v4f){0.f, 0.f, 0.f, 0.f}, 0, 0, 0);
    }
    float* ow = (float*)(smem + L_OW);
    if (quad == 0) {
#pragma unroll
        for (int h = 0; h < 4; ++h) {
            *(v4f*)(ow + (wave * 4 + h) * 128 + 8 * c) = (v4f){o[0][h], o[1][h], o[2][h], o[3][h]};
            *(v4f*)(ow + (wave * 4 + h) * 128 + 8 * c + 4) = (v4f){o[4][h], o[5][h], o[6][h], o[7][h]};
        }
    }
    BAR();
    if (lane < 16) {
        const float* src = ow + (wave >> 1) * 128 + (wave & 1) * 64 + 4 * lane;
        v4f a = *(const v4f*)src;
#pragma unroll
        for (int w = 1; w < 8; ++w) a += *(const v4f*)(src + w * 512);
        ST128(((v4u){f2bf(a[0]) | (f2bf(a[1]) << 16), T, f2bf(a[2]) | (f2bf(a[3]) << 16), T}), o_p2st);
    }
    SCHED();
    wo0 = ldw(wop); wo1 = ldw(wop + 8 * D); w2v = ldw(w2p);
    SCHED();
    float e0, e1;
    float* oq = (float*)(smem + L_OQ) + wave * 32;
    {
        v4u m0 = {0u, 0u, 0u, 0u}, m1 = m0, hg = m0; bool ok0 = false, ok1 = false, okh = false;
        for (unsigned spins = 0;; ++spins) {
            asm volatile("" ::: "memory");
            const v4u t0 = LD128(ok0 ? OOB : o_ml), t1 = LD128(ok1 ? OOB : o_ml + 256u), th = LD128(okh ? OOB : o_hg);
            TAKE(m0, ok0, t0); TAKE(m1, ok1, t1); TAKE(hg, okh, th);
            if (__all(ok0 & ok1 & okh) || spins > SPIN_CAP) break;
        }
        const float MM = max_hi4(fmaxf(u2f(m0[0]), u2f(m1[0])));
        e0 = __expf(u2f(m0[0]) - MM); e1 = __expf(u2f(m1[0]) - MM);
        const float inv = 1.f / sum_hi4(fmaf(e1, u2f(m1[2]), e0 * u2f(m0[2])));
        e0 *= inv; e1 *= inv;
        const float hs = fmaxf(rbf(sum_hi3(u2f((hrow & 1) ? hg[2] : hg[0]))), 0.f);
        if (lane < 8) oq[16 + lane] = lane < 7 ? hs : 0.f;
    }
    float wf[24];
#pragma unroll
    for (int i = 0; i < 4; ++i) { wf[2 * i] = bflo(wo0[i]); wf[2 * i + 1] = bfhi(wo0[i]); wf[8 + 2 * i] = bflo(wo1[i]); wf[9 + 2 * i] = bfhi(wo1[i]); wf[16 + 2 * i] = bflo(w2v[i]); wf[17 + 2 * i] = bfhi(w2v[i]); }
    SCHED();
    {
        v4u p0 = {0u, 0u, 0u, 0u}, p1 = p0; bool ok0 = false, ok1 = false;
        for (unsigned spins = 0;; ++spins) {
            asm volatile("" ::: "memory");
            const v4u t0 = LD128(ok0 ? OOB : o_p2), t1 = LD128(ok1 ? OOB : o_p2 + 1024u);
            TAKE(p0, ok0, t0); TAKE(p1, ok1, t1);
            if (__all(ok0 & ok1) || spins > SPIN_CAP) break;
        }
        const v4f o4 = {rbf(sum_hi4(fmaf(e1, bflo(p1[0]), e0 * bflo(p0[0])))), rbf(sum_hi4(fmaf(e1, bfhi(p1[0]), e0 * bfhi(p0[0])))),
                        rbf(sum_hi4(fmaf(e1, bflo(p1[2]), e0 * bflo(p0[2])))), rbf(sum_hi4(fmaf(e1, bfhi(p1[2]), e0 * bfhi(p0[2]))))};
        if (lane < 4) *(v4f*)(oq + 4 * lane) = o4;
        asm volatile("s_waitcnt lgkmcnt(0)" ::: "memory");
    }
    {
        const float oa = oq[r3], ob = oq[8 + r3], hv = oq[16 + r3];
        float y[8];
#pragma unroll
        for (int i = 0; i < 8; ++i) y[i] = fmaf(hv, wf[16 + i], fmaf(ob, wf[8 + i], oa * wf[i]));
        part[wave * 128 + tcol] = tr_reduce8(y, lane);
    }
    BAR();
    if (wave == 0) {
        if (lane < 32) {
            v2f y = *(const v2f*)(part + 2 * lane);
#pragma unroll
            for (int w = 1; w < 8; ++w) y += *(const v2f*)(part + w * 128 + 2 * lane);
            ST128(((v4u){f2u(y[0]), T, f2u(y[1]), T}), o_p3st);
        }
    }
    if (wave == 1 && b < 32) {
        const unsigned xo = *(const unsigned*)(x + 16 * b + 2 * c3);
        const unsigned o_p3 = WS_P3 + (unsigned)b * 4096u + lane * 16u;
        v4u p0 = {0u, 0u, 0u, 0u}, p1 = p0, p2 = p0, p3 = p0; bool ok0 = false, ok1 = false, ok2 = false, ok3 = false;
        for (unsigned spins = 0;; ++spins) {
            asm volatile("" ::: "memory");
            const v4u t0 = LD128(ok0 ? OOB : o_p3), t1 = LD128(ok1 ? OOB : o_p3 + 1024u), t2 = LD128(ok2 ? OOB : o_p3 + 2048u), t3 = LD128(ok3 ? OOB : o_p3 + 3072u);
            TAKE(p0, ok0, t0); TAKE(p1, ok1, t1); TAKE(p2, ok2, t2); TAKE(p3, ok3, t3);
            if (__all(ok0 & ok1 & ok2 & ok3) || spins > SPIN_CAP) break;
        }
        const float y0 = sum_hi3((u2f(p0[0]) + u2f(p1[0])) + (u2f(p2[0]) + u2f(p3[0])));
        const float y1 = sum_hi3((u2f(p0[2]) + u2f(p1[2])) + (u2f(p2[2]) + u2f(p3[2])));
        if (lane < 8) *(unsigned*)(out + 16 * b + 2 * lane) = f2bf(bflo(xo) + y0) | (f2bf(bfhi(xo) + y1) << 16);
        if (b == 0 && lane == 0) __builtin_amdgcn_raw_buffer_store_b32(T, rs, WS_G, 0, 16);
    }
}

extern "C" void kernel_launch(void* const* d_in, const int* in_sizes, int n_in,
                              void* d_out, int out_size, void* d_ws, size_t ws_size,
                              hipStream_t stream) {
    fused_decode<<<NWG, NTHR, 0, stream>>>((const u16*)d_in[0], (const u16*)d_in[1], (const u16*)d_in[2], (const u16*)d_in[3], (const u16*)d_in[4],
                                           (const u16*)d_in[6], (const u16*)d_in[7], (u16*)d_out, (unsigned char*)d_ws);
}
```
